```python
import torch

def _softmax(x, axis=-1):
    return torch.softmax(x, dim=axis)
def _gelu(x):
    return torch.nn.functional.gelu(x, approximate="tanh")
def _silu(x):
    return torch.nn.functional.silu(x)
def _sigmoid(x):
    return torch.sigmoid(x)
def _layernorm(x, axis=-1, eps=1e-5):
    m = x.mean(dim=axis, keepdim=True)
    v = x.var(dim=axis, keepdim=True, unbiased=False)
    return (x - m) / torch.sqrt(v + eps)
def _scatter_add(tgt, idx, src, axis=0):
    return tgt.clone().index_add_(axis, idx, src)
def _segment_sum(src, seg, n):
    out = torch.zeros((n,) + src.shape[1:], dtype=src.dtype)
    return out.index_add_(0, seg, src)
def _pad(x, pw):
    flat = tuple(v for pair in reversed(pw) for v in pair)
    return torch.nn.functional.pad(x, flat)

kernel_name = 'gated_mix__ssm_conv_s4__dense_activation_squared_relu__prompt'

def setup_inputs():

    _w = (lambda a: (lambda t: t.to(torch.bfloat16) if t.is_floating_point() else t)(torch.as_tensor(a)))
    def _A_setup():
        B, L, D, N = 1, 2048, 512, 128
        A_re = -(lambda _x: torch.exp(torch.as_tensor(_x)))((lambda *s: torch.randn(*s, dtype=torch.bfloat16) * 3.2)(D, N))
        A_im = torch.pi * (lambda *s: torch.randn(*s, dtype=torch.bfloat16) * 0.1)(D, N)
        B_re = (lambda *s: torch.randn(*s, dtype=torch.bfloat16) * 0.2)(D, N)
        B_im = (lambda *s: torch.randn(*s, dtype=torch.bfloat16) * 0.2)(D, N)
        C_re = (lambda *s: torch.randn(*s, dtype=torch.bfloat16) * 0.2)(D, N)
        C_im = (lambda *s: torch.randn(*s, dtype=torch.bfloat16) * 0.2)(D, N)
        log_dt = (lambda *s: torch.randn(*s, dtype=torch.bfloat16) * 0.1)(D) * 0.5 - 4.0
        Dskip = (lambda *s: torch.randn(*s, dtype=torch.bfloat16) * 0.1)(D)
        return {
            "u": _w((lambda *s: torch.randn(*s, dtype=torch.bfloat16) * 0.05)(B, L, D)),
            "A_re": _w(A_re),
            "A_im": _w(A_im),
            "B_re": _w(B_re),
            "B_im": _w(B_im),
            "C_re": _w(C_re),
            "C_im": _w(C_im),
            "log_dt": _w(log_dt),
            "Dskip": _w(Dskip),
        }
    _A_in = _A_setup()

    def _B_setup():
        B, S, D, F = 1, 2048, 512, 1376
        return {
            "x": _w((lambda *s: torch.randn(*s, dtype=torch.bfloat16) * 0.05)(B, S, D)),
            "w1": _w((lambda *s: torch.randn(*s, dtype=torch.bfloat16) * 0.05)(D, F)),
            "b1": _w((lambda *s: torch.randn(*s, dtype=torch.bfloat16) * 0.05)(F)),
            "w2": _w((lambda *s: torch.randn(*s, dtype=torch.bfloat16) * 0.05)(F, D)),
            "b2": _w((lambda *s: torch.randn(*s, dtype=torch.bfloat16) * 0.05)(D)),
        }
    _B_in = _B_setup()

    return {
        "A_u": _A_in["u"],
        "A_A_re": _A_in["A_re"],
        "A_A_im": _A_in["A_im"],
        "A_B_re": _A_in["B_re"],
        "A_B_im": _A_in["B_im"],
        "A_C_re": _A_in["C_re"],
        "A_C_im": _A_in["C_im"],
        "A_log_dt": _A_in["log_dt"],
        "A_Dskip": _A_in["Dskip"],
        "B_x": _B_in["x"],
        "B_w1": _B_in["w1"],
        "B_b1": _B_in["b1"],
        "B_w2": _B_in["w2"],
        "B_b2": _B_in["b2"],
        "gate_w": _w((lambda *s: torch.randn(*s, dtype=torch.bfloat16) * 0.05)(512) * 16.0),
    }

def reference(A_u, A_A_re, A_A_im, A_B_re, A_B_im, A_C_re, A_C_im, A_log_dt, A_Dskip, B_x, B_w1, B_b1, B_w2, B_b2, gate_w):
    def _A_ref(u, A_re, A_im, B_re, B_im, C_re, C_im, log_dt, Dskip):
        Bsz, L, D = u.shape
        dt = torch.exp(log_dt)[:, None]
        A = A_re + 1j * A_im
        Bc = B_re + 1j * B_im
        Cc = C_re + 1j * C_im
        logdA = dt * A
        dB = dt * Bc
        l = (lambda x, dt: x.to(dt))(torch.arange(L), A_re.dtype)
        P = torch.exp(logdA[:, :, None] * l[None, None, :])
        K = torch.einsum("dn,dnl,dn->dl", Cc, P, dB).real
        L2 = 2 * L
        Kp = _pad(K, ((0, 0), (0, L)))
        up = (lambda x, *d: x.permute(*d))(u, 0, 2, 1)
        up = _pad(up, ((0, 0), (0, 0), (0, L)))
        Kf = (lambda x, axes=(-2,-1): torch.fft.rfft2(x.float(), dim=axes))(Kp[:, None, :], axes=(-2, -1))
        Uf = (lambda x, axes=(-2,-1): torch.fft.rfft2(x.float(), dim=axes))(up[:, :, None, :], axes=(-2, -1))
        Yf = Uf * Kf[None, :, :, :]
        y = (lambda x, s, axes=(-2,-1): torch.fft.irfft2(x, s=s, dim=axes).to(torch.bfloat16))(Yf, (1, L2), axes=(-2, -1))[:, :, 0, :L]
        y = (lambda x, *d: x.permute(*d))(y, 0, 2, 1) + u * Dskip[None, None, :]
        return y

    def _B_ref(x, w1, b1, w2, b2):
        h = torch.einsum("bsd,df->bsf", x, w1) + b1
        zero = torch.zeros_like(h)
        r = torch.where(h > zero, h, zero)
        h = r * r
        out = torch.einsum("bsf,fd->bsd", h, w2) + b2
        return out

    x = A_u
    g = _sigmoid((x * gate_w).sum(-1, keepdims=True))
    a = _A_ref(x, A_A_re, A_A_im, A_B_re, A_B_im, A_C_re, A_C_im, A_log_dt, A_Dskip)
    b = _B_ref(x, B_w1, B_b1, B_w2, B_b2)
    return g * a + (1 - g) * b
```

```cpp
#include <hip/hip_runtime.h>
#include <hip/hip_bf16.h>
typedef unsigned short u16;
__device__ __forceinline__ float bf2f(u16 v){ return __uint_as_float(((unsigned)v)<<16); }
__device__ __forceinline__ u16 f2bf(float f){ __hip_bfloat16 b = __float2bfloat16(f); return *reinterpret_cast<u16*>(&b); }
__device__ __forceinline__ float rbf(float f){ return bf2f(f2bf(f)); }
constexpr int L=2048, D=512, N=128, F=1376;

__global__ void kgen(const u16* Are,const u16* Aim,const u16* Bre,const u16* Bim,const u16* Cre,const u16* Cim,const u16* logdt,float* K){
  int d=blockIdx.x;
  float dt=rbf(expf(bf2f(logdt[d])));
  for(int l=threadIdx.x;l<L;l+=blockDim.x){
    float lf=rbf((float)l);
    float s=0;
    for(int n=0;n<N;n++){
      float ar=dt*bf2f(Are[d*N+n]), ai=dt*bf2f(Aim[d*N+n]);
      float br=dt*bf2f(Bre[d*N+n]), bi=dt*bf2f(Bim[d*N+n]);
      float cr=bf2f(Cre[d*N+n]), ci=bf2f(Cim[d*N+n]);
      float e=expf(ar*lf); float sn,cs; sincosf(ai*lf,&sn,&cs);
      float pr=e*cs, pi=e*sn;
      float xr=cr*br-ci*bi, xi=cr*bi+ci*br;
      s+=xr*pr-xi*pi;
    }
    K[d*L+l]=s;
  }
}
__global__ void conv(const u16* u,const float* K,const u16* Dskip,float* a){
  int d=blockIdx.x;
  __shared__ float su[L], sk[L];
  for(int l=threadIdx.x;l<L;l+=blockDim.x){ su[l]=bf2f(u[l*D+d]); sk[l]=K[d*L+l]; }
  __syncthreads();
  float ds=bf2f(Dskip[d]);
  for(int l=threadIdx.x;l<L;l+=blockDim.x){
    float s=0; for(int j=0;j<=l;j++) s+=sk[j]*su[l-j];
    a[l*D+d]=rbf(rbf(s)+rbf(su[l]*ds));
  }
}
__global__ void gemm1(const u16* x,const u16* w1,const u16* b1,u16* h){
  int l=blockIdx.x; __shared__ float sx[D];
  for(int i=threadIdx.x;i<D;i+=blockDim.x) sx[i]=bf2f(x[l*D+i]);
  __syncthreads();
  for(int f=threadIdx.x;f<F;f+=blockDim.x){
    float s=0; for(int k=0;k<D;k++) s+=sx[k]*bf2f(w1[k*F+f]);
    float v=rbf(rbf(s)+bf2f(b1[f])); v=v>0?v:0; h[l*F+f]=f2bf(v*v);
  }
}
__global__ void gemm2(const u16* x,const u16* gw,const u16* h,const u16* w2,const u16* b2,const float* a,u16* out){
  int l=blockIdx.x; __shared__ float sh[F]; __shared__ float red[256];
  for(int i=threadIdx.x;i<F;i+=blockDim.x) sh[i]=bf2f(h[l*F+i]);
  float p=0; for(int i=threadIdx.x;i<D;i+=blockDim.x) p+=rbf(bf2f(x[l*D+i])*bf2f(gw[i]));
  red[threadIdx.x]=p; __syncthreads();
  float t=0; for(int i=0;i<256;i++) t+=red[i];
  t=rbf(t); float g=rbf(1.f/(1.f+expf(-t)));
  for(int d=threadIdx.x;d<D;d+=blockDim.x){
    float s=0; for(int k=0;k<F;k++) s+=sh[k]*bf2f(w2[k*D+d]);
    float b=rbf(rbf(s)+bf2f(b2[d]));
    out[l*D+d]=f2bf(rbf(g*a[l*D+d])+rbf(rbf(1.f-g)*b));
  }
}
extern "C" void kernel_launch(void* const* d_in, const int* in_sizes, int n_in, void* d_out, int out_size, void* d_ws, size_t ws_size, hipStream_t stream) {
  const u16* u=(const u16*)d_in[0];
  float* K=(float*)d_ws; float* a=K+D*L; u16* h=(u16*)(a+D*L);
  kgen<<<D,256,0,stream>>>((const u16*)d_in[1],(const u16*)d_in[2],(const u16*)d_in[3],(const u16*)d_in[4],(const u16*)d_in[5],(const u16*)d_in[6],(const u16*)d_in[7],K);
  conv<<<D,256,0,stream>>>(u,K,(const u16*)d_in[8],a);
  gemm1<<<L,256,0,stream>>>(u,(const u16*)d_in[10],(const u16*)d_in[11],h);
  gemm2<<<L,256,0,stream>>>(u,(const u16*)d_in[14],h,(const u16*)d_in[12],(const u16*)d_in[13],a,(u16*)d_out);
}
```

```cpp
#include <hip/hip_runtime.h>
#include <hip/hip_bf16.h>
typedef unsigned short u16;
__device__ __forceinline__ float bf2f(u16 v){ return __uint_as_float(((unsigned)v)<<16); }
__device__ __forceinline__ u16 f2bf(float f){ __hip_bfloat16 b = __float2bfloat16(f); return *reinterpret_cast<u16*>(&b); }
__device__ __forceinline__ float rbf(float f){ return bf2f(f2bf(f)); }
constexpr int L=2048, D=512, N=128, F=1376;

__global__ void kgen(const u16* Are,const u16* Aim,const u16* Bre,const u16* Bim,const u16* Cre,const u16* Cim,const u16* logdt,float* K){
  int d=blockIdx.x;
  float dt=rbf(expf(bf2f(logdt[d])));
  for(int l=threadIdx.x;l<L;l+=blockDim.x){
    float lf=rbf((float)l);
    float s=0;
    for(int n=0;n<N;n++){
      float ar=dt*bf2f(Are[d*N+n]), ai=dt*bf2f(Aim[d*N+n]);
      float br=dt*bf2f(Bre[d*N+n]), bi=dt*bf2f(Bim[d*N+n]);
      float cr=bf2f(Cre[d*N+n]), ci=bf2f(Cim[d*N+n]);
      float e=expf(ar*lf); float sn,cs; sincosf(ai*lf,&sn,&cs);
      float pr=e*cs, pi=e*sn;
      float xr=cr*br-ci*bi, xi=cr*bi+ci*br;
      s+=xr*pr-xi*pi;
    }
    K[d*L+l]=s;
  }
}
__global__ void conv(const u16* u,const float* K,const u16* Dskip,float* a){
  int d=blockIdx.x;
  __shared__ float su[L], sk[L];
  for(int l=threadIdx.x;l<L;l+=blockDim.x){ su[l]=bf2f(u[l*D+d]); sk[l]=K[d*L+l]; }
  __syncthreads();
  float ds=bf2f(Dskip[d]);
  for(int l=threadIdx.x;l<L;l+=blockDim.x){
    float s=0; for(int j=0;j<=l;j++) s+=sk[j]*su[l-j];
    a[l*D+d]=rbf(rbf(s)+rbf(su[l]*ds));
  }
}
__global__ void gemm1(const u16* x,const u16* w1,const u16* b1,u16* h){
  int l=blockIdx.x; __shared__ float sx[D];
  for(int i=threadIdx.x;i<D;i+=blockDim.x) sx[i]=bf2f(x[l*D+i]);
  __syncthreads();
  for(int f=threadIdx.x;f<F;f+=blockDim.x){
    float s=0; for(int k=0;k<D;k++) s+=sx[k]*bf2f(w1[k*F+f]);
    float v=rbf(rbf(s)+bf2f(b1[f])); v=v>0?v:0; h[l*F+f]=f2bf(v*v);
  }
}
__global__ void gemm2(const u16* x,const u16* gw,const u16* h,const u16* w2,const u16* b2,const float* a,u16* out){
  int l=blockIdx.x; __shared__ float sh[F]; __shared__ float red[256];
  for(int i=threadIdx.x;i<F;i+=blockDim.x) sh[i]=bf2f(h[l*F+i]);
  float p=0; for(int i=threadIdx.x;i<D;i+=blockDim.x) p+=rbf(bf2f(x[l*D+i])*bf2f(gw[i]));
  red[threadIdx.x]=p; __syncthreads();
  float t=0; for(int i=0;i<256;i++) t+=red[i];
  t=rbf(t); float g=rbf(1.f/(1.f+expf(-t)));
  for(int d=threadIdx.x;d<D;d+=blockDim.x){
    float s=0; for(int k=0;k<F;k++) s+=sh[k]*bf2f(w2[k*D+d]);
    float b=rbf(rbf(s)+bf2f(b2[d]));
    out[l*D+d]=f2bf(rbf(g*a[l*D+d])+rbf(rbf(1.f-g)*b));
  }
}
#define MODE 4

typedef unsigned long long u64;
__device__ __forceinline__ u64 ct(){ u64 t; asm volatile("s_memtime %0\n s_waitcnt lgkmcnt(0)":"=s"(t)::"memory"); return t; }
__device__ __forceinline__ u64 rt(){ u64 t; asm volatile("s_memrealtime %0\n s_waitcnt lgkmcnt(0)":"=s"(t)::"memory"); return t; }
#define R8(x) x x x x x x x x
#define KB1 R8(R8("v_fma_f32 %0, %0, %0, %0\n v_fma_f32 %1, %1, %1, %1\n"))
__global__ __launch_bounds__(256,1) void probe(const char* x,const char* w2,u64* st){
  __shared__ __attribute__((aligned(16))) char smem[132*1024];
  int b=blockIdx.x, tid=threadIdx.x, lane=tid&63; int w=__builtin_amdgcn_readfirstlane(tid>>6);
  u64 r0=rt(); u64 c0=ct(); u64 val=0;
  int xcd=b&7, idx=b>>3; int ti=(xcd>>1)*4+(idx&3);
  if(MODE==1){ unsigned ft; asm volatile("global_load_dword %0, %1, off\n s_waitcnt vmcnt(0)":"=v"(ft):"v"(x+(size_t)b*8192+w*2048):"memory"); val=ct()-c0; }
  if(MODE==2||MODE==5){
    const char* p = MODE==2 ? x+(size_t)(ti*128)*1024+lane*16 : w2+(size_t)(idx>>2)*128*1024+lane*16;
    #pragma unroll
    for(int i=0;i<32;i++) __builtin_amdgcn_global_load_lds((const unsigned*)(p+(i*4+w)*1024),(__attribute__((address_space(3))) unsigned*)(smem+(i*4+w)*1024),16,0,0);
    asm volatile("s_waitcnt vmcnt(0)":::"memory"); val=ct()-c0;
    __syncthreads(); if(smem[tid]==123 && smem[tid+1000]==77 && val==1) val=2;
  }
  if(MODE==3){ float a=tid,bb=1.f;
    #pragma unroll
    for(int i=0;i<4;i++){ asm volatile(KB1 KB1 KB1 KB1 : "+v"(a), "+v"(bb)); }
    val=ct()-c0; }
  if(MODE==4) val=r0;
  if(MODE==6){
    unsigned ft; asm volatile("global_load_dword %0, %1, off\n s_waitcnt vmcnt(0)":"=v"(ft):"v"(x+(size_t)b*8192+w*2048):"memory"); u64 c1=ct();
    asm volatile("global_load_dword %0, %1, off\n s_waitcnt vmcnt(0)":"=v"(ft):"v"(x+(size_t)b*8192+w*2048+1024):"memory"); val=ct()-c1; }
  if(lane==0) st[b*4+w]=val;
}
__global__ void spin(const u64* st,u64* sink){
  __shared__ u64 v[1024]; int t=threadIdx.x; v[t]=st[t]; __syncthreads();
  u64 me=v[t]; int r=0; for(int i=0;i<1024;i++) r+= (v[i]<me)||(v[i]==me&&i<t);
  __shared__ u64 s[1024]; s[r]=me; __syncthreads();
  if(t==0){ u64 stat = MODE==4 ? (s[1023]-s[0])*24 : s[512]; if(stat>100000) stat=100000;
    u64 r0=rt(); u64 dur=stat*10;   while(rt()-r0<dur) __builtin_amdgcn_s_sleep(8); sink[0]=stat; }
}
extern "C" void kernel_launch(void* const* d_in, const int* in_sizes, int n_in, void* d_out, int out_size, void* d_ws, size_t ws_size, hipStream_t stream) {
  const u16* u=(const u16*)d_in[0];
  float* K=(float*)d_ws; float* a=K+D*L; u16* h=(u16*)(a+D*L);
  u64* st=(u64*)((char*)d_ws+(64u<<20));
  probe<<<256,256,0,stream>>>((const char*)d_in[0],(const char*)d_in[12],st);
  spin<<<1,1024,0,stream>>>(st,st+2048);
  kgen<<<D,256,0,stream>>>((const u16*)d_in[1],(const u16*)d_in[2],(const u16*)d_in[3],(const u16*)d_in[4],(const u16*)d_in[5],(const u16*)d_in[6],(const u16*)d_in[7],K);
  conv<<<D,256,0,stream>>>(u,K,(const u16*)d_in[8],a);
  gemm1<<<L,256,0,stream>>>(u,(const u16*)d_in[10],(const u16*)d_in[11],h);
  gemm2<<<L,256,0,stream>>>(u,(const u16*)d_in[14],h,(const u16*)d_in[12],(const u16*)d_in[13],a,(u16*)d_out);
}
```

```cpp
#include <hip/hip_runtime.h>
#include <stdint.h>

struct Srd { const void* p; uint32_t n, f; };
struct Args { Srd s[16]; };

__global__ __launch_bounds__(512, 2) void gated_mix_fused(Args)
{
  __shared__ __attribute__((aligned(16))) char lds[163840];
  asm volatile(
    "s_load_dwordx16 s[16:31], s[0:1], 0x0\n"
    "s_load_dwordx16 s[64:79], s[0:1], 0xc0\n"
    "v_lshrrev_b32 v4, 6, v0\n"
    "s_getpc_b64 s[10:11]\n"
    ".Lpc_%=:\n"
    "v_readfirstlane_b32 s13, v4\n"
    "v_lshlrev_b32 v8, 7, v0\n"
    "v_min_u32 v8, .Lend_%=-.Lpc_%=-4, v8\n"
    "v_and_b32 v1, 63, v0\n"
    "global_load_dword a127, v8, s[10:11]\n"
    "v_lshrrev_b32 v9, 3, v1\n"
    "v_and_b32 v4, 3, v4\n"
    "s_and_b32 s3, s13, 3\n"
    "s_cmp_lt_u32 s13, 4\n"
    "s_cbranch_scc1 .LG_%=\n"
    "s_load_dwordx16 s[32:47], s[0:1], 0x40\n"
    "s_load_dwordx16 s[48:63], s[0:1], 0x80\n"
    "v_and_b32 v8, 255, v0\n"
    "v_lshlrev_b32 v8, 1, v8\n"
    "s_lshl_b32 s12, s2, 9\n"
    "s_lshl_b32 s13, s2, 2\n"
    "v_mov_b32 v24, 0\n"
    "v_mov_b32 v25, 0\n"
    "v_mov_b32 v26, 0\n"
    "v_mov_b32 v27, 0\n"
    "v_mov_b32 v28, 0\n"
    "v_mov_b32 v29, 0\n"
    "v_lshrrev_b32 v5, 4, v1\n"
    "v_lshlrev_b32 v6, 4, v1\n"
    "v_mov_b32 v7, 0x80000000\n"
    "v_and_b32 v2, 31, v1\n"
    "v_lshrrev_b32 v3, 5, v1\n"
    "s_waitcnt lgkmcnt(0)\n"
    "buffer_load_short_d16_hi v24, v8, s[32:35], s12 offen\n"
    "buffer_load_short_d16_hi v25, v8, s[36:39], s12 offen\n"
    "buffer_load_short_d16_hi v26, v8, s[40:43], s12 offen\n"
    "buffer_load_short_d16_hi v27, v8, s[44:47], s12 offen\n"
    "buffer_load_short_d16_hi v28, v8, s[48:51], s12 offen\n"
    "buffer_load_short_d16_hi v29, v8, s[52:55], s12 offen\n"
    "s_load_dword s14, s[56:57], s13\n"
    "s_load_dword s15, s[60:61], s13\n"
    "s_load_dword s80, s[24:25], 0x0\n"
    "v_bfe_u32 v10, v1, 4, 1\n"
    "v_cmp_eq_u32 vcc, v10, v3\n"
    "v_and_b32 v10, 1, v1\n"
    "v_lshlrev_b32 v10, 4, v10\n"
    "v_mov_b32 v11, 0x3f80\n"
    "v_lshlrev_b32 v10, v10, v11\n"
    "v_cndmask_b32 v10, 0, v10, vcc\n"
    "v_bfe_u32 v11, v1, 1, 3\n"
    "v_cmp_eq_u32 vcc, 0, v11\n"
    "s_nop 1\n"
    "v_cndmask_b32 v9, 0, v10, vcc\n"
    "v_accvgpr_write_b32 a16, v9\n"
    "v_cmp_eq_u32 vcc, 1, v11\n"
    "s_nop 1\n"
    "v_cndmask_b32 v9, 0, v10, vcc\n"
    "v_accvgpr_write_b32 a17, v9\n"
    "v_cmp_eq_u32 vcc, 2, v11\n"
    "s_nop 1\n"
    "v_cndmask_b32 v9, 0, v10, vcc\n"
    "v_accvgpr_write_b32 a18, v9\n"
    "v_cmp_eq_u32 vcc, 3, v11\n"
    "s_nop 1\n"
    "v_cndmask_b32 v9, 0, v10, vcc\n"
    "v_accvgpr_write_b32 a19, v9\n"
    "v_cmp_eq_u32 vcc, 4, v11\n"
    "s_nop 1\n"
    "v_cndmask_b32 v9, 0, v10, vcc\n"
    "v_accvgpr_write_b32 a20, v9\n"
    "v_cmp_eq_u32 vcc, 5, v11\n"
    "s_nop 1\n"
    "v_cndmask_b32 v9, 0, v10, vcc\n"
    "v_accvgpr_write_b32 a21, v9\n"
    "v_cmp_eq_u32 vcc, 6, v11\n"
    "s_nop 1\n"
    "v_cndmask_b32 v9, 0, v10, vcc\n"
    "v_accvgpr_write_b32 a22, v9\n"
    "v_cmp_eq_u32 vcc, 7, v11\n"
    "s_nop 1\n"
    "v_cndmask_b32 v9, 0, v10, vcc\n"
    "v_accvgpr_write_b32 a23, v9\n"
    "s_lshr_b32 s12, s3, 1\n"
    "s_mul_i32 s41, s12, 6912\n"
    "s_add_u32 s41, s41, 25984\n"
    "s_waitcnt lgkmcnt(0)\n"
    "s_lshl_b32 s12, s12, 4\n"
    "s_lshr_b32 s13, s14, s12\n"
    "s_lshr_b32 s12, s15, s12\n"
    "v_lshlrev_b32 v30, 16, s13\n"
    "v_lshlrev_b32 v13, 16, s12\n"
    "s_and_b32 s14, s2, 7\n"
    "s_lshr_b32 s15, s2, 3\n"
    "s_lshr_b32 s8, s15, 2\n"
    "s_and_b32 s9, s15, 3\n"
    "s_lshl_b32 s7, s14, 2\n"
    "s_or_b32 s7, s7, s9\n"
    "s_lshr_b32 s4, s14, 1\n"
    "s_lshl_b32 s4, s4, 2\n"
    "s_or_b32 s4, s4, s9\n"
    "s_and_b32 s5, s14, 1\n"
    "s_lshl_b32 s5, s5, 3\n"
    "s_or_b32 s5, s5, s8\n"
    "v_and_b32 v8, 3, v1\n"
    "v_lshl_add_u32 v8, v5, 2, v8\n"
    "v_bfe_u32 v9, v1, 2, 2\n"
    "v_lshl_add_u32 v8, v8, 3, v9\n"
    "s_and_b32 s12, s5, 1\n"
    "s_lshl_b32 s12, s12, 2\n"
    "s_or_b32 s12, s12, s3\n"
    "v_bfe_u32 v9, v8, 1, 3\n"
    "v_xor_b32 v9, s12, v9\n"
    "v_lshlrev_b32 v9, 4, v9\n"
    "v_lshl_or_b32 v8, v8, 7, v9\n"
    "v_xor_b32 v9, 32, v8\n"
    "v_add_u32 v16, 48000, v8\n"
    "v_add_u32 v17, 48512, v9\n"
    "s_lshl_b32 s12, s5, 2\n"
    "s_add_u32 s12, s12, s3\n"
    "s_lshl_b32 s12, s12, 15\n"
    "s_lshl_b32 s13, s4, 10\n"
    "s_add_u32 s12, s12, s13\n"
    "s_add_u32 s63, s12, 163840\n"
    "s_add_u32 s43, s63, 16384\n"
    "s_lshl_b32 s13, s5, 4\n"
    "s_add_u32 s13, s13, s4\n"
    "s_lshl_b32 s13, s13, 3\n"
    "s_add_u32 s13, s13, 4096\n"
    "s_mov_b32 s62, s13\n"
    "v_mul_f32 v30, 0x3fb8aa3b, v30\n"
    "v_exp_f32 v30, v30\n"
    "s_add_u32 s80, s80, 1\n"
    "v_mov_b32 v12, s80\n"
    "v_cvt_pk_bf16_f32 v30, v30, v30\n"
    "v_lshlrev_b32 v30, 16, v30\n"
    "s_waitcnt vmcnt(0)\n"
    "v_mul_f32 v24, v30, v24\n"
    "v_mul_f32 v25, v30, v25\n"
    "v_mul_f32 v26, v30, v26\n"
    "v_mul_f32 v27, v30, v27\n"
    "v_mul_f32 v42, 0x4138aa3b, v24\n"
    "v_mul_f32 v43, 0x3fa2f983, v25\n"
    "v_exp_f32 v42, v42\n"
    "v_fract_f32 v43, v43\n"
    "v_cos_f32 v38, v43\n"
    "v_sin_f32 v39, v43\n"
    "v_mul_f32 v44, 0x3fb8aa3b, v24\n"
    "v_mul_f32 v45, 0x3e22f983, v25\n"
    "v_exp_f32 v44, v44\n"
    "v_fract_f32 v45, v45\n"
    "v_cos_f32 v32, v45\n"
    "v_sin_f32 v33, v45\n"
    "v_mul_f32 v34, 0x4338aa3b, v24\n"
    "v_mul_f32 v35, 0x41a2f983, v25\n"
    "v_exp_f32 v34, v34\n"
    "v_fract_f32 v35, v35\n"
    "v_cos_f32 v40, v35\n"
    "v_sin_f32 v41, v35\n"
    "v_mul_f32 v46, v28, v26\n"
    "v_mul_f32 v47, v28, v27\n"
    "v_fma_f32 v46, -v29, v27, v46\n"
    "v_fma_f32 v47, -v29, v26, -v47\n"
    "v_mul_f32 v38, v42, v38\n"
    "v_mul_f32 v39, v42, v39\n"
    "v_mul_f32 v32, v44, v32\n"
    "v_mul_f32 v33, v44, v33\n"
    "v_mul_f32 v40, v34, v40\n"
    "v_mul_f32 v41, v34, v41\n"
    "v_pk_mul_f32 v[42:43], v[38:39], v[38:39] op_sel:[0,0] op_sel_hi:[0,1]\n"
    "v_pk_mul_f32 v[44:45], v[32:33], v[32:33] op_sel:[0,0] op_sel_hi:[0,1]\n"
    "v_pk_fma_f32 v[36:37], v[38:39], v[38:39], v[42:43] op_sel:[1,1,0] op_sel_hi:[1,0,1] neg_lo:[0,1,0]\n"
    "v_pk_fma_f32 v[34:35], v[32:33], v[32:33], v[44:45] op_sel:[1,1,0] op_sel_hi:[1,0,1] neg_lo:[0,1,0]\n"
    "v_cvt_pk_bf16_f32 v80, 1.0, v38\n"
    "v_cvt_pk_bf16_f32 v84, 0, v39\n"
    "v_pk_mul_f32 v[42:43], v[38:39], v[36:37] op_sel:[0,0] op_sel_hi:[0,1]\n"
    "v_pk_mul_f32 v[44:45], v[36:37], v[36:37] op_sel:[0,0] op_sel_hi:[0,1]\n"
    "v_pk_fma_f32 v[28:29], v[38:39], v[36:37], v[42:43] op_sel:[1,1,0] op_sel_hi:[1,0,1] neg_lo:[0,1,0]\n"
    "v_pk_fma_f32 v[26:27], v[36:37], v[36:37], v[44:45] op_sel:[1,1,0] op_sel_hi:[1,0,1] neg_lo:[0,1,0]\n"
    "v_cvt_pk_bf16_f32 v81, v36, v28\n"
    "v_cvt_pk_bf16_f32 v85, v37, v29\n"
    "v_pk_mul_f32 v[42:43], v[28:29], v[36:37] op_sel:[0,0] op_sel_hi:[0,1]\n"
    "v_pk_mul_f32 v[44:45], v[46:47], v[40:41] op_sel:[0,0] op_sel_hi:[0,1] neg_hi:[0,1]\n"
    "v_pk_fma_f32 v[28:29], v[28:29], v[36:37], v[42:43] op_sel:[1,1,0] op_sel_hi:[1,0,1] neg_lo:[0,1,0]\n"
    "v_pk_fma_f32 v[30:31], v[46:47], v[40:41], v[44:45] op_sel:[1,1,0] op_sel_hi:[1,0,1]\n"
    "v_cvt_pk_bf16_f32 v82, v26, v28\n"
    "v_cvt_pk_bf16_f32 v86, v27, v29\n"
    "v_pk_mul_f32 v[42:43], v[26:27], v[36:37] op_sel:[0,0] op_sel_hi:[0,1]\n"
    "v_pk_mul_f32 v[44:45], v[28:29], v[36:37] op_sel:[0,0] op_sel_hi:[0,1]\n"
    "v_pk_fma_f32 v[26:27], v[26:27], v[36:37], v[42:43] op_sel:[1,1,0] op_sel_hi:[1,0,1] neg_lo:[0,1,0]\n"
    "v_pk_fma_f32 v[28:29], v[28:29], v[36:37], v[44:45] op_sel:[1,1,0] op_sel_hi:[1,0,1] neg_lo:[0,1,0]\n"
    "v_cvt_pk_bf16_f32 v83, v26, v28\n"
    "v_cvt_pk_bf16_f32 v87, v27, v29\n"
    "v_pk_mul_f32 v[42:43], v[26:27], v[36:37] op_sel:[0,0] op_sel_hi:[0,1]\n"
    "v_pk_mul_f32 v[44:45], v[28:29], v[36:37] op_sel:[0,0] op_sel_hi:[0,1]\n"
    "v_pk_fma_f32 v[26:27], v[26:27], v[36:37], v[42:43] op_sel:[1,1,0] op_sel_hi:[1,0,1] neg_lo:[0,1,0]\n"
    "v_pk_fma_f32 v[28:29], v[28:29], v[36:37], v[44:45] op_sel:[1,1,0] op_sel_hi:[1,0,1] neg_lo:[0,1,0]\n"
    "v_cvt_pk_bf16_f32 v88, v26, v28\n"
    "v_cvt_pk_bf16_f32 v92, v27, v29\n"
    "v_pk_mul_f32 v[42:43], v[26:27], v[36:37] op_sel:[0,0] op_sel_hi:[0,1]\n"
    "v_pk_mul_f32 v[44:45], v[28:29], v[36:37] op_sel:[0,0] op_sel_hi:[0,1]\n"
    "v_pk_fma_f32 v[26:27], v[26:27], v[36:37], v[42:43] op_sel:[1,1,0] op_sel_hi:[1,0,1] neg_lo:[0,1,0]\n"
    "v_pk_fma_f32 v[28:29], v[28:29], v[36:37], v[44:45] op_sel:[1,1,0] op_sel_hi:[1,0,1] neg_lo:[0,1,0]\n"
    "v_cvt_pk_bf16_f32 v89, v26, v28\n"
    "v_cvt_pk_bf16_f32 v93, v27, v29\n"
    "v_pk_mul_f32 v[42:43], v[26:27], v[36:37] op_sel:[0,0] op_sel_hi:[0,1]\n"
    "v_pk_mul_f32 v[44:45], v[28:29], v[36:37] op_sel:[0,0] op_sel_hi:[0,1]\n"
    "v_pk_fma_f32 v[26:27], v[26:27], v[36:37], v[42:43] op_sel:[1,1,0] op_sel_hi:[1,0,1] neg_lo:[0,1,0]\n"
    "v_pk_fma_f32 v[28:29], v[28:29], v[36:37], v[44:45] op_sel:[1,1,0] op_sel_hi:[1,0,1] neg_lo:[0,1,0]\n"
    "v_cvt_pk_bf16_f32 v90, v26, v28\n"
    "v_cvt_pk_bf16_f32 v94, v27, v29\n"
    "v_pk_mul_f32 v[42:43], v[26:27], v[36:37] op_sel:[0,0] op_sel_hi:[0,1]\n"
    "v_pk_mul_f32 v[44:45], v[28:29], v[36:37] op_sel:[0,0] op_sel_hi:[0,1]\n"
    "v_pk_fma_f32 v[26:27], v[26:27], v[36:37], v[42:43] op_sel:[1,1,0] op_sel_hi:[1,0,1] neg_lo:[0,1,0]\n"
    "v_pk_fma_f32 v[28:29], v[28:29], v[36:37], v[44:45] op_sel:[1,1,0] op_sel_hi:[1,0,1] neg_lo:[0,1,0]\n"
    "v_cvt_pk_bf16_f32 v91, v26, v28\n"
    "v_cvt_pk_bf16_f32 v95, v27, v29\n"
    "v_mfma_f32_32x32x16_bf16 v[48:63], v[80:83], a[16:19], 0\n"
    "v_mul_f32 v42, v46, v32\n"
    "v_mul_f32 v43, v46, v33\n"
    "v_mul_f32 v44, v30, v32\n"
    "v_mul_f32 v45, v30, v33\n"
    "v_fma_f32 v24, v47, v33, v42\n"
    "v_fma_f32 v25, v47, v32, -v43\n"
    "v_mfma_f32_32x32x16_bf16 v[64:79], v[84:87], a[16:19], 0\n"
    "v_fma_f32 v28, v31, v33, v44\n"
    "v_fma_f32 v29, v31, v32, -v45\n"
    "v_cvt_pk_bf16_f32 v96, v46, v24\n"
    "v_cvt_pk_bf16_f32 v100, v47, v25\n"
    "v_cvt_pk_bf16_f32 v104, v30, v28\n"
    "v_cvt_pk_bf16_f32 v108, v31, v29\n"
    "v_mfma_f32_32x32x16_bf16 v[48:63], v[88:91], a[20:23], v[48:63]\n"
    "v_mul_f32 v42, v30, v34\n"
    "v_mul_f32 v43, v30, v35\n"
    "v_mul_f32 v44, v30, v40\n"
    "v_mul_f32 v45, v30, v41\n"
    "v_fma_f32 v26, v31, v35, v42\n"
    "v_fma_f32 v27, v31, v34, -v43\n"
    "v_mfma_f32_32x32x16_bf16 v[64:79], v[92:95], a[20:23], v[64:79]\n"
    "v_fma_f32 v30, v31, v41, v44\n"
    "v_fma_f32 v31, v31, v40, -v45\n"
    "v_mul_f32 v42, v46, v34\n"
    "v_mul_f32 v43, v46, v35\n"
    "v_mul_f32 v44, v24, v34\n"
    "v_mul_f32 v45, v24, v35\n"
    "v_fma_f32 v46, v47, v35, v42\n"
    "v_fma_f32 v47, v47, v34, -v43\n"
    "v_fma_f32 v24, v25, v35, v44\n"
    "v_fma_f32 v25, v25, v34, -v45\n"
    "v_cvt_pk_bf16_f32 v97, v46, v24\n"
    "v_cvt_pk_bf16_f32 v101, v47, v25\n"
    "v_pk_mul_f32 v[42:43], v[34:35], v[34:35] op_sel:[0,0] op_sel_hi:[0,1]\n"
    "v_pk_mul_f32 v[44:45], v[28:29], v[34:35] op_sel:[0,0] op_sel_hi:[0,1] neg_hi:[0,1]\n"
    "v_pk_fma_f32 v[36:37], v[34:35], v[34:35], v[42:43] op_sel:[1,1,0] op_sel_hi:[1,0,1] neg_lo:[0,1,0]\n"
    "v_pk_fma_f32 v[28:29], v[28:29], v[34:35], v[44:45] op_sel:[1,1,0] op_sel_hi:[1,0,1]\n"
    "v_cvt_pk_bf16_f32 v105, v26, v28\n"
    "v_cvt_pk_bf16_f32 v109, v27, v29\n"
    "v_pk_mul_f32 v[42:43], v[46:47], v[34:35] op_sel:[0,0] op_sel_hi:[0,1] neg_hi:[0,1]\n"
    "v_pk_mul_f32 v[44:45], v[24:25], v[34:35] op_sel:[0,0] op_sel_hi:[0,1] neg_hi:[0,1]\n"
    "v_pk_fma_f32 v[46:47], v[46:47], v[34:35], v[42:43] op_sel:[1,1,0] op_sel_hi:[1,0,1]\n"
    "v_pk_fma_f32 v[24:25], v[24:25], v[34:35], v[44:45] op_sel:[1,1,0] op_sel_hi:[1,0,1]\n"
    "v_cvt_pk_bf16_f32 v98, v46, v24\n"
    "v_cvt_pk_bf16_f32 v102, v47, v25\n"
    "v_pk_mul_f32 v[42:43], v[26:27], v[34:35] op_sel:[0,0] op_sel_hi:[0,1] neg_hi:[0,1]\n"
    "v_pk_mul_f32 v[44:45], v[28:29], v[34:35] op_sel:[0,0] op_sel_hi:[0,1] neg_hi:[0,1]\n"
    "v_pk_fma_f32 v[26:27], v[26:27], v[34:35], v[42:43] op_sel:[1,1,0] op_sel_hi:[1,0,1]\n"
    "v_pk_fma_f32 v[28:29], v[28:29], v[34:35], v[44:45] op_sel:[1,1,0] op_sel_hi:[1,0,1]\n"
    "v_cvt_pk_bf16_f32 v106, v26, v28\n"
    "v_cvt_pk_bf16_f32 v110, v27, v29\n"
    "v_pk_mul_f32 v[42:43], v[46:47], v[34:35] op_sel:[0,0] op_sel_hi:[0,1] neg_hi:[0,1]\n"
    "v_pk_mul_f32 v[44:45], v[24:25], v[34:35] op_sel:[0,0] op_sel_hi:[0,1] neg_hi:[0,1]\n"
    "v_pk_fma_f32 v[46:47], v[46:47], v[34:35], v[42:43] op_sel:[1,1,0] op_sel_hi:[1,0,1]\n"
    "v_pk_fma_f32 v[24:25], v[24:25], v[34:35], v[44:45] op_sel:[1,1,0] op_sel_hi:[1,0,1]\n"
    "v_cvt_pk_bf16_f32 v99, v46, v24\n"
    "v_cvt_pk_bf16_f32 v103, v47, v25\n"
    "v_pk_mul_f32 v[42:43], v[26:27], v[34:35] op_sel:[0,0] op_sel_hi:[0,1] neg_hi:[0,1]\n"
    "v_pk_mul_f32 v[44:45], v[28:29], v[34:35] op_sel:[0,0] op_sel_hi:[0,1] neg_hi:[0,1]\n"
    "v_pk_fma_f32 v[26:27], v[26:27], v[34:35], v[42:43] op_sel:[1,1,0] op_sel_hi:[1,0,1]\n"
    "v_pk_fma_f32 v[28:29], v[28:29], v[34:35], v[44:45] op_sel:[1,1,0] op_sel_hi:[1,0,1]\n"
    "v_cvt_pk_bf16_f32 v107, v26, v28\n"
    "v_cvt_pk_bf16_f32 v111, v27, v29\n"
    "v_cvt_pk_bf16_f32 v80, v48, v64\n"
    "v_cvt_pk_bf16_f32 v81, v49, v65\n"
    "v_cvt_pk_bf16_f32 v82, v50, v66\n"
    "v_cvt_pk_bf16_f32 v83, v51, v67\n"
    "v_cvt_pk_bf16_f32 v84, v52, v68\n"
    "v_cvt_pk_bf16_f32 v85, v53, v69\n"
    "v_cvt_pk_bf16_f32 v86, v54, v70\n"
    "v_cvt_pk_bf16_f32 v87, v55, v71\n"
    "v_cvt_pk_bf16_f32 v88, v56, v72\n"
    "v_cvt_pk_bf16_f32 v89, v57, v73\n"
    "v_cvt_pk_bf16_f32 v90, v58, v74\n"
    "v_cvt_pk_bf16_f32 v91, v59, v75\n"
    "v_cvt_pk_bf16_f32 v92, v60, v76\n"
    "v_cvt_pk_bf16_f32 v93, v61, v77\n"
    "v_cvt_pk_bf16_f32 v94, v62, v78\n"
    "v_cvt_pk_bf16_f32 v95, v63, v79\n"
    "v_mfma_f32_32x32x16_bf16 v[48:63], v[96:99], a[16:19], 0\n"
    "v_mul_f32 v42, v30, v34\n"
    "v_mul_f32 v43, v30, v35\n"
    "v_mul_f32 v44, v30, v40\n"
    "v_mul_f32 v45, v30, v41\n"
    "v_fma_f32 v24, v31, v35, v42\n"
    "v_fma_f32 v25, v31, v34, -v43\n"
    "v_mfma_f32_32x32x16_bf16 v[64:79], v[100:103], a[16:19], 0\n"
    "v_fma_f32 v26, v31, v41, v44\n"
    "v_fma_f32 v27, v31, v40, -v45\n"
    "v_cvt_pk_bf16_f32 v112, v30, v24\n"
    "v_cvt_pk_bf16_f32 v116, v31, v25\n"
    "v_mul_f32 v42, v26, v34\n"
    "v_mul_f32 v43, v26, v35\n"
    "v_mfma_f32_32x32x16_bf16 v[48:63], v[104:107], a[20:23], v[48:63]\n"
    "v_mul_f32 v44, v26, v40\n"
    "v_mul_f32 v45, v26, v41\n"
    "v_fma_f32 v28, v27, v35, v42\n"
    "v_fma_f32 v29, v27, v34, -v43\n"
    "v_fma_f32 v46, v27, v41, v44\n"
    "v_fma_f32 v47, v27, v40, -v45\n"
    "v_mfma_f32_32x32x16_bf16 v[64:79], v[108:111], a[20:23], v[64:79]\n"
    "v_cvt_pk_bf16_f32 v114, v26, v28\n"
    "v_cvt_pk_bf16_f32 v118, v27, v29\n"
    "v_mul_f32 v42, v30, v36\n"
    "v_mul_f32 v43, v30, v37\n"
    "v_mul_f32 v44, v24, v36\n"
    "v_mul_f32 v45, v24, v37\n"
    "v_fma_f32 v30, v31, v37, v42\n"
    "v_fma_f32 v31, v31, v36, -v43\n"
    "v_fma_f32 v24, v25, v37, v44\n"
    "v_fma_f32 v25, v25, v36, -v45\n"
    "v_cvt_pk_bf16_f32 v113, v30, v24\n"
    "v_cvt_pk_bf16_f32 v117, v31, v25\n"
    "v_pk_mul_f32 v[42:43], v[26:27], v[36:37] op_sel:[0,0] op_sel_hi:[0,1] neg_hi:[0,1]\n"
    "v_pk_mul_f32 v[44:45], v[28:29], v[36:37] op_sel:[0,0] op_sel_hi:[0,1] neg_hi:[0,1]\n"
    "v_pk_fma_f32 v[26:27], v[26:27], v[36:37], v[42:43] op_sel:[1,1,0] op_sel_hi:[1,0,1]\n"
    "v_pk_fma_f32 v[28:29], v[28:29], v[36:37], v[44:45] op_sel:[1,1,0] op_sel_hi:[1,0,1]\n"
    "v_cvt_pk_bf16_f32 v115, v26, v28\n"
    "v_cvt_pk_bf16_f32 v119, v27, v29\n"
    "v_mul_f32 v42, v46, v36\n"
    "v_mul_f32 v43, v46, v37\n"
    "v_mul_f32 v44, v46, v40\n"
    "v_mul_f32 v45, v46, v41\n"
    "v_fma_f32 v24, v47, v37, v42\n"
    "v_fma_f32 v25, v47, v36, -v43\n"
    "v_fma_f32 v26, v47, v41, v44\n"
    "v_fma_f32 v27, v47, v40, -v45\n"
    "v_cvt_pk_bf16_f32 v120, v46, v24\n"
    "v_cvt_pk_bf16_f32 v124, v47, v25\n"
    "v_mul_f32 v42, v26, v36\n"
    "v_mul_f32 v43, v26, v37\n"
    "v_mul_f32 v44, v26, v40\n"
    "v_mul_f32 v45, v26, v41\n"
    "v_fma_f32 v28, v27, v37, v42\n"
    "v_fma_f32 v29, v27, v36, -v43\n"
    "v_fma_f32 v46, v27, v41, v44\n"
    "v_fma_f32 v47, v27, v40, -v45\n"
    "v_cvt_pk_bf16_f32 v121, v26, v28\n"
    "v_cvt_pk_bf16_f32 v125, v27, v29\n"
    "v_mul_f32 v42, v46, v36\n"
    "v_mul_f32 v43, v46, v37\n"
    "v_mul_f32 v44, v46, v40\n"
    "v_mul_f32 v45, v46, v41\n"
    "v_fma_f32 v24, v47, v37, v42\n"
    "v_fma_f32 v25, v47, v36, -v43\n"
    "v_fma_f32 v26, v47, v41, v44\n"
    "v_fma_f32 v27, v47, v40, -v45\n"
    "v_cvt_pk_bf16_f32 v122, v46, v24\n"
    "v_cvt_pk_bf16_f32 v126, v47, v25\n"
    "v_mul_f32 v42, v26, v36\n"
    "v_mul_f32 v43, v26, v37\n"
    "v_mul_f32 v44, v26, v40\n"
    "v_mul_f32 v45, v26, v41\n"
    "v_fma_f32 v28, v27, v37, v42\n"
    "v_fma_f32 v29, v27, v36, -v43\n"
    "v_fma_f32 v46, v27, v41, v44\n"
    "v_fma_f32 v47, v27, v40, -v45\n"
    "v_cvt_pk_bf16_f32 v123, v26, v28\n"
    "v_cvt_pk_bf16_f32 v127, v27, v29\n"
    "v_cvt_pk_bf16_f32 v96, v48, v64\n"
    "v_cvt_pk_bf16_f32 v97, v49, v65\n"
    "v_cvt_pk_bf16_f32 v98, v50, v66\n"
    "v_cvt_pk_bf16_f32 v99, v51, v67\n"
    "v_cvt_pk_bf16_f32 v100, v52, v68\n"
    "v_cvt_pk_bf16_f32 v101, v53, v69\n"
    "v_cvt_pk_bf16_f32 v102, v54, v70\n"
    "v_cvt_pk_bf16_f32 v103, v55, v71\n"
    "v_cvt_pk_bf16_f32 v104, v56, v72\n"
    "v_cvt_pk_bf16_f32 v105, v57, v73\n"
    "v_cvt_pk_bf16_f32 v106, v58, v74\n"
    "v_cvt_pk_bf16_f32 v107, v59, v75\n"
    "v_cvt_pk_bf16_f32 v108, v60, v76\n"
    "v_cvt_pk_bf16_f32 v109, v61, v77\n"
    "v_cvt_pk_bf16_f32 v110, v62, v78\n"
    "v_cvt_pk_bf16_f32 v111, v63, v79\n"
    "v_mfma_f32_16x16x32_bf16 a[0:3], v[96:99], v[80:83], 0\n"
    "v_mfma_f32_16x16x32_bf16 a[0:3], v[100:103], v[84:87], a[0:3]\n"
    "v_mfma_f32_16x16x32_bf16 a[0:3], v[104:107], v[88:91], a[0:3]\n"
    "v_mfma_f32_16x16x32_bf16 a[0:3], v[108:111], v[92:95], a[0:3]\n"
    "s_waitcnt lgkmcnt(0)\n"
    "s_barrier\n"
    "ds_read_b64_tr_b16 a[32:33], v16 offset:0\n"
    "ds_read_b64_tr_b16 a[34:35], v17 offset:0\n"
    "ds_read_b64_tr_b16 a[36:37], v16 offset:8\n"
    "ds_read_b64_tr_b16 a[38:39], v17 offset:8\n"
    "v_mfma_f32_32x32x16_bf16 v[48:63], v[112:115], a[16:19], 0\n"
    "s_and_b32 s12, s3, 1\n"
    "s_lshl_b32 s12, s12, 2\n"
    "s_add_u32 s12, s12, s41\n"
    "v_and_b32 v10, 15, v1\n"
    "v_bfe_u32 v11, v1, 4, 1\n"
    "v_mul_u32_u24 v18, 72, v10\n"
    "v_lshl_add_u32 v18, v11, 5, v18\n"
    "v_mul_u32_u24 v11, 1152, v3\n"
    "v_add3_u32 v18, v18, v11, s12\n"
    "v_mfma_f32_32x32x16_bf16 v[64:79], v[116:119], a[16:19], 0\n"
    "v_mul_f32 v42, v46, v40\n"
    "s_waitcnt lgkmcnt(0)\n"
    "buffer_store_dwordx4 a[32:35], v6, s[24:27], s63 offen sc1\n"
    "buffer_store_dwordx4 a[36:39], v6, s[24:27], s43 offen sc1\n"
    "v_mul_f32 v43, v46, v41\n"
    "v_mul_f32 v44, v40, v40\n"
    "v_mul_f32 v45, v40, v41\n"
    "v_fma_f32 v24, v47, v41, v42\n"
    "v_fma_f32 v25, v47, v40, -v43\n"
    "v_mfma_f32_32x32x16_bf16 v[48:63], v[120:123], a[20:23], v[48:63]\n"
    "v_fma_f32 v34, -v41, v41, v44\n"
    "v_fma_f32 v35, v41, v40, v45\n"
    "v_cvt_pk_bf16_f32 v96, v46, v24\n"
    "v_cvt_pk_bf16_f32 v100, v47, v25\n"
    "v_mul_f32 v42, v46, v34\n"
    "v_mul_f32 v43, v46, v35\n"
    "v_mfma_f32_32x32x16_bf16 v[64:79], v[124:127], a[20:23], v[64:79]\n"
    "v_mul_f32 v44, v24, v34\n"
    "v_mul_f32 v45, v24, v35\n"
    "v_fma_f32 v46, v47, v35, v42\n"
    "v_fma_f32 v47, v47, v34, -v43\n"
    "v_fma_f32 v24, v25, v35, v44\n"
    "v_fma_f32 v25, v25, v34, -v45\n"
    "v_cvt_pk_bf16_f32 v97, v46, v24\n"
    "v_cvt_pk_bf16_f32 v101, v47, v25\n"
    "v_mul_f32 v42, v46, v34\n"
    "v_mul_f32 v43, v46, v35\n"
    "v_mul_f32 v44, v24, v34\n"
    "v_mul_f32 v45, v24, v35\n"
    "v_fma_f32 v46, v47, v35, v42\n"
    "v_fma_f32 v47, v47, v34, -v43\n"
    "v_fma_f32 v24, v25, v35, v44\n"
    "v_fma_f32 v25, v25, v34, -v45\n"
    "v_cvt_pk_bf16_f32 v98, v46, v24\n"
    "v_cvt_pk_bf16_f32 v102, v47, v25\n"
    "v_mul_f32 v42, v46, v34\n"
    "v_mul_f32 v43, v46, v35\n"
    "v_mul_f32 v44, v24, v34\n"
    "v_mul_f32 v45, v24, v35\n"
    "v_fma_f32 v46, v47, v35, v42\n"
    "v_fma_f32 v47, v47, v34, -v43\n"
    "v_fma_f32 v24, v25, v35, v44\n"
    "v_fma_f32 v25, v25, v34, -v45\n"
    "v_cvt_pk_bf16_f32 v99, v46, v24\n"
    "v_cvt_pk_bf16_f32 v103, v47, v25\n"
    "v_mul_f32 v42, v46, v34\n"
    "v_mul_f32 v43, v46, v35\n"
    "s_waitcnt lgkmcnt(0)\n"
    "s_barrier\n"
    "v_fma_f32 v46, v47, v35, v42\n"
    "v_fma_f32 v47, v47, v34, -v43\n"
    "v_cvt_pk_bf16_f32 v104, v46, v46\n"
    "v_cvt_pk_bf16_f32 v108, v47, v47\n"
    "v_mov_b32 v105, 0\n"
    "v_mov_b32 v109, 0\n"
    "v_mov_b32 v106, 0\n"
    "v_mov_b32 v110, 0\n"
    "v_mov_b32 v107, 0\n"
    "v_mov_b32 v111, 0\n"
    "v_cvt_pk_bf16_f32 v112, v48, v64\n"
    "v_cvt_pk_bf16_f32 v113, v49, v65\n"
    "v_cvt_pk_bf16_f32 v114, v50, v66\n"
    "v_cvt_pk_bf16_f32 v115, v51, v67\n"
    "v_cvt_pk_bf16_f32 v116, v52, v68\n"
    "v_cvt_pk_bf16_f32 v117, v53, v69\n"
    "v_cvt_pk_bf16_f32 v118, v54, v70\n"
    "v_cvt_pk_bf16_f32 v119, v55, v71\n"
    "v_cvt_pk_bf16_f32 v120, v56, v72\n"
    "v_cvt_pk_bf16_f32 v121, v57, v73\n"
    "v_cvt_pk_bf16_f32 v122, v58, v74\n"
    "v_cvt_pk_bf16_f32 v123, v59, v75\n"
    "v_cvt_pk_bf16_f32 v124, v60, v76\n"
    "v_cvt_pk_bf16_f32 v125, v61, v77\n"
    "v_cvt_pk_bf16_f32 v126, v62, v78\n"
    "v_cvt_pk_bf16_f32 v127, v63, v79\n"
    "v_mfma_f32_16x16x32_bf16 a[4:7], v[112:115], v[80:83], 0\n"
    "v_mfma_f32_16x16x32_bf16 a[4:7], v[116:119], v[84:87], a[4:7]\n"
    "v_mfma_f32_16x16x32_bf16 a[4:7], v[120:123], v[88:91], a[4:7]\n"
    "v_mfma_f32_16x16x32_bf16 a[4:7], v[124:127], v[92:95], a[4:7]\n"
    "v_mfma_f32_32x32x16_bf16 v[48:63], v[96:99], a[16:19], 0\n"
    "v_mul_u32_u24 v21, 576, v5\n"
    "v_add_u32 v21, s12, v21\n"
    "v_sub_u32 v11, 2, v3\n"
    "v_lshlrev_b32 v11, v11, v10\n"
    "v_lshrrev_b32 v19, 3, v11\n"
    "v_add_u32 v11, v11, v19\n"
    "v_mfma_f32_32x32x16_bf16 v[64:79], v[100:103], a[16:19], 0\n"
    "v_lshl_add_u32 v19, v11, 3, v21\n"
    "v_add_u32 v19, 2304, v19\n"
    "v_mfma_f32_32x32x16_bf16 v[48:63], v[104:107], a[20:23], v[48:63]\n"
    "v_mul_u32_u24 v11, 272, v3\n"
    "v_add3_u32 v20, v19, v11, 16\n"
    "v_lshrrev_b32 v11, 3, v10\n"
    "v_mfma_f32_32x32x16_bf16 v[64:79], v[108:111], a[20:23], v[64:79]\n"
    "v_add_u32 v10, v10, v11\n"
    "v_lshl_add_u32 v21, v10, 3, v21\n"
    "s_nop 9\n"
    "v_cvt_pk_bf16_f32 v96, v48, v64\n"
    "v_cvt_pk_bf16_f32 v97, v49, v65\n"
    "v_cvt_pk_bf16_f32 v98, v50, v66\n"
    "v_cvt_pk_bf16_f32 v99, v51, v67\n"
    "s_waitcnt lgkmcnt(0)\n"
    "s_waitcnt vmcnt(0)\n"
    "s_barrier\n"
    "s_cmp_lg_u32 s3, 0\n"
    "s_cbranch_scc1 .Lskip1_%=\n"
    "s_mov_b64 exec, 1\n"
    "buffer_store_dword v12, off, s[24:27], s62 sc1\n"
    "s_mov_b64 exec, -1\n"
    ".Lskip1_%=:\n"
    "v_cvt_pk_bf16_f32 v100, v52, v68\n"
    "v_cvt_pk_bf16_f32 v101, v53, v69\n"
    "v_cvt_pk_bf16_f32 v102, v54, v70\n"
    "v_cvt_pk_bf16_f32 v103, v55, v71\n"
    "v_cvt_pk_bf16_f32 v104, v56, v72\n"
    "v_cvt_pk_bf16_f32 v105, v57, v73\n"
    "v_cvt_pk_bf16_f32 v106, v58, v74\n"
    "v_cvt_pk_bf16_f32 v107, v59, v75\n"
    "v_cvt_pk_bf16_f32 v108, v60, v76\n"
    "v_cvt_pk_bf16_f32 v109, v61, v77\n"
    "v_cvt_pk_bf16_f32 v110, v62, v78\n"
    "v_cvt_pk_bf16_f32 v111, v63, v79\n"
    "v_mfma_f32_16x16x32_bf16 a[8:11], v[96:99], v[80:83], 0\n"
    "v_mfma_f32_16x16x32_bf16 a[8:11], v[100:103], v[84:87], a[8:11]\n"
    "v_mfma_f32_16x16x32_bf16 a[8:11], v[104:107], v[88:91], a[8:11]\n"
    "v_mfma_f32_16x16x32_bf16 a[8:11], v[108:111], v[92:95], a[8:11]\n"
    "v_and_b32 v22, 3, v1\n"
    "v_lshlrev_b32 v22, 2, v22\n"
    "v_add_u32 v22, 162688, v22\n"
    "v_lshlrev_b32 v23, 2, v4\n"
    "v_add_u32 v23, 162688, v23\n"
    "s_lshr_b32 s13, s2, 4\n"
    "s_lshl_b32 s13, s13, 7\n"
    "s_add_u32 s13, s13, 4096\n"
    "v_and_b32 v25, 15, v1\n"
    "v_lshl_add_u32 v25, v25, 3, s13\n"
    "v_lshrrev_b32 v8, 1, v1\n"
    "v_lshl_add_u32 v8, v4, 6, v8\n"
    "v_and_b32 v9, 1, v1\n"
    "v_lshlrev_b32 v26, 6, v8\n"
    "v_lshl_or_b32 v26, v9, 4, v26\n"
    "v_mul_u32_u24 v9, 4672, v9\n"
    "v_lshl_add_u32 v27, v8, 4, v9\n"
    "v_add_u32 v27, 17152, v27\n"
    "s_lshl_b32 s45, s2, 13\n"
    "s_add_u32 s45, s45, 2260992\n"
    "s_lshr_b32 s44, s2, 1\n"
    "s_lshl_b32 s44, s44, 14\n"
    "s_and_b32 s13, s2, 1\n"
    "s_lshl_b32 s13, s13, 5\n"
    "s_add_u32 s44, s44, s13\n"
    "s_add_u32 s44, s44, 163840\n"
    "v_mov_b32 v24, 0\n"
    "ds_write2_b32 v23, v24, v24 offset0:0 offset1:4\n"
    "v_and_b32 v8, 127, v0\n"
    "v_lshlrev_b32 v9, 1, v8\n"
    "v_sub_u32 v28, 255, v9\n"
    "v_ashrrev_i32 v11, 3, v28\n"
    "v_add_u32 v10, v28, v11\n"
    "v_lshl_add_u32 v31, v10, 3, s41\n"
    "v_bfe_u32 v10, v28, 1, 1\n"
    "v_add3_u32 v10, v10, v28, 0\n"
    "v_ashrrev_i32 v10, 1, v10\n"
    "v_lshrrev_b32 v11, 3, v10\n"
    "v_add_u32 v10, v10, v11\n"
    "v_lshl_add_u32 v34, v10, 3, s41\n"
    "v_bfe_u32 v10, v28, 2, 1\n"
    "v_add3_u32 v10, v10, v28, 1\n"
    "v_ashrrev_i32 v10, 2, v10\n"
    "v_lshrrev_b32 v11, 3, v10\n"
    "v_add_u32 v10, v10, v11\n"
    "v_lshl_add_u32 v37, v10, 3, s41\n"
    "v_bfe_u32 v10, v28, 3, 1\n"
    "v_add3_u32 v10, v10, v28, 3\n"
    "v_ashrrev_i32 v10, 3, v10\n"
    "v_lshrrev_b32 v11, 3, v10\n"
    "v_add_u32 v10, v10, v11\n"
    "v_lshl_add_u32 v40, v10, 3, s41\n"
    "v_sub_u32 v29, 254, v9\n"
    "v_ashrrev_i32 v11, 3, v29\n"
    "s_waitcnt lgkmcnt(0)\n"
    "s_barrier\n"
    "v_add_u32 v10, v29, v11\n"
    "v_lshl_add_u32 v32, v10, 3, s41\n"
    "v_bfe_u32 v10, v29, 1, 1\n"
    "v_add3_u32 v10, v10, v29, 0\n"
    "v_ashrrev_i32 v10, 1, v10\n"
    "v_lshrrev_b32 v11, 3, v10\n"
    "v_add_u32 v10, v10, v11\n"
    "v_lshl_add_u32 v35, v10, 3, s41\n"
    "v_bfe_u32 v10, v29, 2, 1\n"
    "v_add3_u32 v10, v10, v29, 1\n"
    "v_ashrrev_i32 v10, 2, v10\n"
    "v_lshrrev_b32 v11, 3, v10\n"
    "v_add_u32 v10, v10, v11\n"
    "v_lshl_add_u32 v38, v10, 3, s41\n"
    "v_bfe_u32 v10, v29, 3, 1\n"
    "v_add3_u32 v10, v10, v29, 3\n"
    "v_ashrrev_i32 v10, 3, v10\n"
    "v_lshrrev_b32 v11, 3, v10\n"
    "v_add_u32 v10, v10, v11\n"
    "v_lshl_add_u32 v41, v10, 3, s41\n"
    "v_sub_u32 v30, 253, v9\n"
    "v_ashrrev_i32 v11, 3, v30\n"
    "v_add_u32 v10, v30, v11\n"
    "v_lshl_add_u32 v33, v10, 3, s41\n"
    "v_bfe_u32 v10, v30, 1, 1\n"
    "v_add3_u32 v10, v10, v30, 0\n"
    "v_ashrrev_i32 v10, 1, v10\n"
    "v_lshrrev_b32 v11, 3, v10\n"
    "v_add_u32 v10, v10, v11\n"
    "v_lshl_add_u32 v36, v10, 3, s41\n"
    "v_bfe_u32 v10, v30, 2, 1\n"
    "v_add3_u32 v10, v10, v30, 1\n"
    "v_ashrrev_i32 v10, 2, v10\n"
    "v_lshrrev_b32 v11, 3, v10\n"
    "v_add_u32 v10, v10, v11\n"
    "v_lshl_add_u32 v39, v10, 3, s41\n"
    "v_bfe_u32 v10, v30, 3, 1\n"
    "v_add3_u32 v10, v10, v30, 3\n"
    "v_ashrrev_i32 v10, 3, v10\n"
    "v_lshrrev_b32 v11, 3, v10\n"
    "v_add_u32 v10, v10, v11\n"
    "v_lshl_add_u32 v42, v10, 3, s41\n"
    "v_ashrrev_i32 v10, 31, v30\n"
    "v_lshl_add_u32 v36, v10, 4, v36\n"
    "s_lshr_b32 s12, s3, 1\n"
    "s_mul_i32 s12, s12, 8320\n"
    "v_lshl_add_u32 v43, v8, 2, s12\n"
    "v_add_u32 v44, 4160, v43\n"
    "s_waitcnt lgkmcnt(0)\n"
    "s_barrier\n"
    "buffer_load_dword v14, v25, s[24:27], 0 offen sc1\n"
    "s_waitcnt lgkmcnt(0)\n"
    "s_barrier\n"
    "s_mov_b64 s[10:11], vcc\n"
    "s_waitcnt vmcnt(0)\n"
    "v_cmp_ne_u32 vcc, s80, v14\n"
    "s_nop 1\n"
    "s_cbranch_vccnz .Lfuc_slow_%=\n"
    ".Lfuc_ok_%=:\n"
    "buffer_load_dwordx4 v[56:59], v26, s[24:27], s44 offen offset:0 sc1\n"
    "buffer_load_dwordx4 v[60:63], v26, s[24:27], s44 offen offset:2048 sc1\n"
    "s_mov_b64 vcc, s[10:11]\n"
    "s_waitcnt lgkmcnt(0)\n"
    "s_barrier\n"
    "ds_write2_b32 v18, a0, a1 offset0:0 offset1:2\n"
    "ds_write2_b32 v18, a2, a3 offset0:4 offset1:6\n"
    "ds_write2_b32 v19, a4, a5 offset0:0 offset1:2\n"
    "ds_write2_b32 v20, a6, a7 offset0:0 offset1:2\n"
    "v_add_u32 v21, 4608, v21\n"
    "ds_write2_b32 v21, a8, a9 offset0:0 offset1:36\n"
    "ds_write2_b32 v21, a10, a11 offset0:72 offset1:108\n"
    "s_waitcnt lgkmcnt(0)\n"
    "s_barrier\n"
    "v_mov_b32 v18, 0\n"
    "s_lshr_b32 s12, s3, 1\n"
    "v_and_b32 v8, 15, v1\n"
    "s_and_b32 s13, s3, 1\n"
    "s_mul_i32 s14, s12, 4672\n"
    "s_add_u32 s14, s14, 16672\n"
    "v_lshl_add_u32 v19, v8, 5, s14\n"
    "v_lshl_add_u32 v19, v5, 4, v19\n"
    "ds_read_b64 v[80:81], v40 offset:5472\n"
    "ds_read_b64 v[82:83], v41 offset:5472\n"
    "ds_read_b64 v[84:85], v42 offset:5472\n"
    "ds_read_b64 v[86:87], v40 offset:5184\n"
    "ds_read_b64 v[88:89], v41 offset:5184\n"
    "ds_read_b64 v[90:91], v42 offset:5184\n"
    "ds_read_b64 v[92:93], v40 offset:4896\n"
    "ds_read_b64 v[94:95], v41 offset:4896\n"
    "ds_read_b64 v[96:97], v42 offset:4896\n"
    "ds_read_b64 v[98:99], v40 offset:4608\n"
    "ds_read_b64 v[100:101], v41 offset:4608\n"
    "ds_read_b64 v[102:103], v42 offset:4608\n"
    "s_mul_i32 s14, s12, 8320\n"
    "s_add_u32 s14, s14, 7260\n"
    "v_and_b32 v9, 1, v1\n"
    "v_mul_u32_u24 v9, 4160, v9\n"
    "v_sub_u32 v9, s14, v9\n"
    "v_lshl_add_u32 v9, v5, 4, v9\n"
    "v_lshrrev_b32 v8, 1, v8\n"
    "v_lshlrev_b32 v8, 2, v8\n"
    "v_sub_u32 v20, v9, v8\n"
    "v_subrev_u32 v21, 1024, v20\n"
    "v_subrev_u32 v45, 2048, v20\n"
    "v_subrev_u32 v46, 3072, v20\n"
    "ds_read_b64 v[104:105], v37 offset:4032\n"
    "ds_read_b64 v[106:107], v38 offset:4032\n"
    "ds_read_b64 v[108:109], v39 offset:4032\n"
    "ds_read_b64 v[110:111], v37 offset:3456\n"
    "ds_read_b64 v[112:113], v38 offset:3456\n"
    "ds_read_b64 v[114:115], v39 offset:3456\n"
    "ds_read_b64 v[116:117], v34 offset:2304\n"
    "ds_read_b64 v[118:119], v35 offset:2304\n"
    "ds_read_b64 v[120:121], v36 offset:2304\n"
    "ds_read_b64 v[122:123], v31 offset:0\n"
    "ds_read_b64 v[124:125], v32 offset:0\n"
    "ds_read_b64 v[126:127], v33 offset:0\n"
    "v_cmp_gt_i32 vcc, 0, v30\n"
    "s_waitcnt lgkmcnt(15)\n"
    "v_add_f32 v80, v80, v81\n"
    "v_add_f32 v82, v82, v83\n"
    "v_add_f32 v84, v84, v85\n"
    "v_cvt_pk_bf16_f32 v64, v80, v82\n"
    "v_cvt_pk_bf16_f32 v65, v82, v84\n"
    "v_add_f32 v86, v86, v87\n"
    "v_add_f32 v88, v88, v89\n"
    "v_add_f32 v90, v90, v91\n"
    "v_cvt_pk_bf16_f32 v66, v86, v88\n"
    "v_cvt_pk_bf16_f32 v67, v88, v90\n"
    "ds_write2st64_b32 v43, v64, v66 offset0:0 offset1:2\n"
    "ds_write2st64_b32 v44, v65, v67 offset0:0 offset1:2\n"
    "v_add_f32 v92, v92, v93\n"
    "v_add_f32 v94, v94, v95\n"
    "v_add_f32 v96, v96, v97\n"
    "v_cvt_pk_bf16_f32 v68, v92, v94\n"
    "s_waitcnt lgkmcnt(0)\n"
    "s_barrier\n"
    "v_cvt_pk_bf16_f32 v69, v94, v96\n"
    "v_add_f32 v98, v98, v99\n"
    "v_add_f32 v100, v100, v101\n"
    "v_add_f32 v102, v102, v103\n"
    "v_cvt_pk_bf16_f32 v70, v98, v100\n"
    "v_cvt_pk_bf16_f32 v71, v100, v102\n"
    "ds_write2st64_b32 v43, v68, v70 offset0:4 offset1:6\n"
    "ds_write2st64_b32 v44, v69, v71 offset0:4 offset1:6\n"
    "v_add_f32 v104, v104, v105\n"
    "v_add_f32 v106, v106, v107\n"
    "v_add_f32 v108, v108, v109\n"
    "v_cvt_pk_bf16_f32 v72, v104, v106\n"
    "v_cvt_pk_bf16_f32 v73, v106, v108\n"
    "v_add_f32 v110, v110, v111\n"
    "v_add_f32 v112, v112, v113\n"
    "v_add_f32 v114, v114, v115\n"
    "v_cvt_pk_bf16_f32 v74, v110, v112\n"
    "v_cvt_pk_bf16_f32 v75, v112, v114\n"
    "ds_write2st64_b32 v43, v72, v74 offset0:8 offset1:10\n"
    "ds_write2st64_b32 v44, v73, v75 offset0:8 offset1:10\n"
    "v_add_f32 v116, v116, v117\n"
    "v_add_f32 v118, v118, v119\n"
    "v_add_f32 v120, v120, v121\n"
    "v_cvt_pk_bf16_f32 v76, v116, v118\n"
    "v_cvt_pk_bf16_f32 v77, v118, v120\n"
    "v_add_f32 v122, v122, v123\n"
    "v_add_f32 v124, v124, v125\n"
    "v_add_f32 v126, v126, v127\n"
    "v_cndmask_b32 v126, v126, v18, vcc\n"
    "v_cndmask_b32 v13, 0, v13, vcc\n"
    "v_add_f32 v124, v124, v13\n"
    "v_cvt_pk_bf16_f32 v78, v122, v124\n"
    "v_cvt_pk_bf16_f32 v79, v124, v126\n"
    "ds_write2st64_b32 v43, v76, v78 offset0:12 offset1:14\n"
    "ds_write2st64_b32 v44, v77, v79 offset0:12 offset1:14\n"
    "s_mul_i32 s14, s3, 4160\n"
    "s_add_u32 s14, s14, 4096\n"
    "v_and_b32 v8, 7, v1\n"
    "v_lshl_add_u32 v8, v8, 2, s14\n"
    "ds_write_b32 v8, v18\n"
    "s_mul_i32 s13, s12, 4160\n"
    "s_lshl_b32 s14, s3, 8\n"
    "s_add_u32 s13, s13, s14\n"
    "s_add_u32 s13, s13, 16640\n"
    "v_lshl_add_u32 v8, v1, 2, s13\n"
    "ds_write_b32 v8, v18\n"
    "s_waitcnt vmcnt(0)\n"
    "ds_write_b128 v27, v[56:59] offset:0\n"
    "ds_write_b128 v27, v[60:63] offset:512\n"
    "v_mov_b32 v24, 1\n"
    "ds_write_b32 v23, v24 offset:16\n"
    "ds_read_b32 v24, v22 offset:16\n"
    "s_waitcnt lgkmcnt(0)\n"
    "v_cmp_gt_u32 vcc, 1, v24\n"
    "s_nop 1\n"
    "s_cbranch_vccnz .Lc2_slow_%=\n"
    ".Lc2_ok_%=:\n"
    "s_lshr_b32 s12, s3, 1\n"
    "s_and_b32 s13, s3, 1\n"
    "s_lshl_b32 s15, s13, 12\n"
    "v_and_b32 v8, 15, v1\n"
    "v_lshlrev_b32 v8, 5, v8\n"
    "v_lshl_add_u32 v8, v3, 4, v8\n"
    "s_lshl_b32 s14, s13, 10\n"
    "s_sub_u32 s14, 1536, s14\n"
    "v_bfe_u32 v9, v1, 4, 1\n"
    "v_mad_u32_u24 v8, v9, s14, v8\n"
    "s_lshl_b32 s14, s12, 3\n"
    "s_add_u32 s14, s14, s13\n"
    "s_lshl_b32 s14, s14, 9\n"
    "v_add_u32 v16, s14, v8\n"
    "s_lshl_b32 s14, s2, 1\n"
    "s_add_u32 s14, s14, s12\n"
    "s_lshl_b32 s14, s14, 3\n"
    "s_add_u32 s14, s14, s15\n"
    "s_add_u32 s62, s14, 8192\n"
    "s_lshl_b32 s14, s8, 1\n"
    "s_add_u32 s14, s14, s13\n"
    "s_lshl_b32 s14, s14, 8\n"
    "s_add_u32 s15, s7, 4\n"
    "s_bfe_u32 s15, s15, 0x10003\n"
    "s_lshl_b32 s15, s15, 12\n"
    "s_add_u32 s14, s14, s15\n"
    "s_add_u32 s14, s14, 8192\n"
    "v_lshl_add_u32 v13, v2, 3, s14\n"
    "s_lshl_b32 s14, s7, 1\n"
    "s_add_u32 s14, s14, s12\n"
    "s_lshr_b32 s15, s14, 2\n"
    "s_lshl_b32 s15, s15, 6\n"
    "s_and_b32 s14, s14, 3\n"
    "s_add_u32 s14, s14, s15\n"
    "s_lshl_b32 s14, s14, 7\n"
    "s_add_u32 s14, s14, 32768\n"
    "v_writelane_b32 v13, s14, 63\n"
    "s_waitcnt lgkmcnt(0)\n"
    "s_bitcmp1_b32 s3, 0\n"
    "s_cbranch_scc1 .Lodd_%=\n"
    "ds_read_b128 a[0:3], v19 offset:448\n"
    "ds_read2_b32 a[64:65], v20 offset0:240 offset1:241\n"
    "ds_read2_b32 a[66:67], v20 offset0:242 offset1:243\n"
    "ds_read_b128 a[4:7], v19 offset:960\n"
    "ds_read2_b32 a[68:69], v20 offset0:112 offset1:113\n"
    "ds_read2_b32 a[70:71], v20 offset0:114 offset1:115\n"
    "ds_read_b128 a[8:11], v19 offset:1472\n"
    "ds_read2_b32 a[72:73], v21 offset0:240 offset1:241\n"
    "ds_read2_b32 a[74:75], v21 offset0:242 offset1:243\n"
    "ds_read_b128 a[12:15], v19 offset:1984\n"
    "ds_read2_b32 a[76:77], v21 offset0:112 offset1:113\n"
    "ds_read2_b32 a[78:79], v21 offset0:114 offset1:115\n"
    "ds_read_b128 a[16:19], v19 offset:2496\n"
    "ds_read2_b32 a[80:81], v45 offset0:240 offset1:241\n"
    "ds_read2_b32 a[82:83], v45 offset0:242 offset1:243\n"
    "ds_read_b128 a[20:23], v19 offset:3008\n"
    "ds_read2_b32 a[84:85], v45 offset0:112 offset1:113\n"
    "ds_read2_b32 a[86:87], v45 offset0:114 offset1:115\n"
    "ds_read_b128 a[24:27], v19 offset:3520\n"
    "ds_read2_b32 a[88:89], v46 offset0:240 offset1:241\n"
    "ds_read2_b32 a[90:91], v46 offset0:242 offset1:243\n"
    "ds_read_b128 a[28:31], v19 offset:4032\n"
    "ds_read2_b32 a[92:93], v46 offset0:112 offset1:113\n"
    "ds_read2_b32 a[94:95], v46 offset0:114 offset1:115\n"
    "s_waitcnt lgkmcnt(15)\n"
    "v_mfma_f32_16x16x32_bf16 v[112:115], a[64:67], a[0:3], 0\n"
    "v_mfma_f32_16x16x32_bf16 v[116:119], a[68:71], a[8:11], 0\n"
    "ds_read_b128 a[32:35], v19 offset:384\n"
    "ds_read2_b32 a[96:97], v20 offset0:224 offset1:225\n"
    "v_mfma_f32_16x16x32_bf16 v[120:123], a[72:75], a[8:11], 0\n"
    "ds_read2_b32 a[98:99], v20 offset0:226 offset1:227\n"
    "s_waitcnt lgkmcnt(12)\n"
    "v_mfma_f32_16x16x32_bf16 v[124:127], a[76:79], a[16:19], 0\n"
    "ds_read_b128 a[36:39], v19 offset:896\n"
    "ds_read2_b32 a[100:101], v20 offset0:96 offset1:97\n"
    "v_mfma_f32_16x16x32_bf16 v[116:119], a[72:75], a[4:7], v[116:119]\n"
    "ds_read2_b32 a[102:103], v20 offset0:98 offset1:99\n"
    "v_mfma_f32_16x16x32_bf16 v[120:123], a[68:71], a[12:15], v[120:123]\n"
    "ds_read_b128 a[40:43], v19 offset:1408\n"
    "ds_read2_b32 a[104:105], v21 offset0:224 offset1:225\n"
    "v_mfma_f32_16x16x32_bf16 v[124:127], a[80:83], a[12:15], v[124:127]\n"
    "ds_read2_b32 a[106:107], v21 offset0:226 offset1:227\n"
    "v_mfma_f32_16x16x32_bf16 v[116:119], a[64:67], a[12:15], v[116:119]\n"
    "ds_read_b128 a[44:47], v19 offset:1920\n"
    "v_mfma_f32_16x16x32_bf16 v[120:123], a[76:79], a[4:7], v[120:123]\n"
    "ds_read2_b32 a[108:109], v21 offset0:96 offset1:97\n"
    "ds_read2_b32 a[110:111], v21 offset0:98 offset1:99\n"
    "s_waitcnt lgkmcnt(15)\n"
    "v_mfma_f32_16x16x32_bf16 v[124:127], a[72:75], a[20:23], v[124:127]\n"
    "ds_read_b128 a[48:51], v19 offset:2432\n"
    "v_mfma_f32_16x16x32_bf16 v[116:119], a[76:79], a[0:3], v[116:119]\n"
    "ds_read2_b32 a[112:113], v45 offset0:224 offset1:225\n"
    "ds_read2_b32 a[114:115], v45 offset0:226 offset1:227\n"
    "v_mfma_f32_16x16x32_bf16 v[120:123], a[64:67], a[16:19], v[120:123]\n"
    "ds_read_b128 a[52:55], v19 offset:2944\n"
    "v_mfma_f32_16x16x32_bf16 v[124:127], a[84:87], a[8:11], v[124:127]\n"
    "ds_read2_b32 a[116:117], v45 offset0:96 offset1:97\n"
    "v_mfma_f32_16x16x32_bf16 v[120:123], a[80:83], a[0:3], v[120:123]\n"
    "ds_read2_b32 a[118:119], v45 offset0:98 offset1:99\n"
    "ds_read_b128 a[56:59], v19 offset:3456\n"
    "v_mfma_f32_16x16x32_bf16 v[124:127], a[68:71], a[24:27], v[124:127]\n"
    "ds_read2_b32 a[120:121], v46 offset0:224 offset1:225\n"
    "v_mfma_f32_16x16x32_bf16 v[124:127], a[88:91], a[4:7], v[124:127]\n"
    "ds_read2_b32 a[122:123], v46 offset0:226 offset1:227\n"
    "ds_read_b128 a[60:63], v19 offset:3968\n"
    "s_waitcnt lgkmcnt(15)\n"
    "v_mfma_f32_16x16x32_bf16 v[124:127], a[64:67], a[28:31], v[124:127]\n"
    "ds_read2_b32 a[124:125], v46 offset0:96 offset1:97\n"
    "v_mfma_f32_16x16x32_bf16 v[124:127], a[92:95], a[0:3], v[124:127]\n"
    "ds_read2_b32 a[126:127], v46 offset0:98 offset1:99\n"
    "v_mfma_f32_16x16x32_bf16 v[112:115], a[96:99], a[32:35], v[112:115]\n"
    "s_waitcnt lgkmcnt(15)\n"
    "v_mfma_f32_16x16x32_bf16 v[116:119], a[100:103], a[40:43], v[116:119]\n"
    "ds_read_b128 a[0:3], v19 offset:320\n"
    "ds_read2_b32 a[64:65], v20 offset0:208 offset1:209\n"
    "v_mfma_f32_16x16x32_bf16 v[120:123], a[104:107], a[40:43], v[120:123]\n"
    "ds_read2_b32 a[66:67], v20 offset0:210 offset1:211\n"
    "s_waitcnt lgkmcnt(12)\n"
    "v_mfma_f32_16x16x32_bf16 v[124:127], a[108:111], a[48:51], v[124:127]\n"
    "ds_read_b128 a[4:7], v19 offset:832\n"
    "ds_read2_b32 a[68:69], v20 offset0:80 offset1:81\n"
    "v_mfma_f32_16x16x32_bf16 v[116:119], a[104:107], a[36:39], v[116:119]\n"
    "ds_read2_b32 a[70:71], v20 offset0:82 offset1:83\n"
    "v_mfma_f32_16x16x32_bf16 v[120:123], a[100:103], a[44:47], v[120:123]\n"
    "ds_read_b128 a[8:11], v19 offset:1344\n"
    "ds_read2_b32 a[72:73], v21 offset0:208 offset1:209\n"
    "v_mfma_f32_16x16x32_bf16 v[124:127], a[112:115], a[44:47], v[124:127]\n"
    "ds_read2_b32 a[74:75], v21 offset0:210 offset1:211\n"
    "v_mfma_f32_16x16x32_bf16 v[116:119], a[96:99], a[44:47], v[116:119]\n"
    "ds_read_b128 a[12:15], v19 offset:1856\n"
    "v_mfma_f32_16x16x32_bf16 v[120:123], a[108:111], a[36:39], v[120:123]\n"
    "ds_read2_b32 a[76:77], v21 offset0:80 offset1:81\n"
    "ds_read2_b32 a[78:79], v21 offset0:82 offset1:83\n"
    "s_waitcnt lgkmcnt(15)\n"
    "v_mfma_f32_16x16x32_bf16 v[124:127], a[104:107], a[52:55], v[124:127]\n"
    "ds_read_b128 a[16:19], v19 offset:2368\n"
    "v_mfma_f32_16x16x32_bf16 v[116:119], a[108:111], a[32:35], v[116:119]\n"
    "ds_read2_b32 a[80:81], v45 offset0:208 offset1:209\n"
    "ds_read2_b32 a[82:83], v45 offset0:210 offset1:211\n"
    "v_mfma_f32_16x16x32_bf16 v[120:123], a[96:99], a[48:51], v[120:123]\n"
    "ds_read_b128 a[20:23], v19 offset:2880\n"
    "v_mfma_f32_16x16x32_bf16 v[124:127], a[116:119], a[40:43], v[124:127]\n"
    "ds_read2_b32 a[84:85], v45 offset0:80 offset1:81\n"
    "v_mfma_f32_16x16x32_bf16 v[120:123], a[112:115], a[32:35], v[120:123]\n"
    "ds_read2_b32 a[86:87], v45 offset0:82 offset1:83\n"
    "ds_read_b128 a[24:27], v19 offset:3392\n"
    "v_mfma_f32_16x16x32_bf16 v[124:127], a[100:103], a[56:59], v[124:127]\n"
    "ds_read2_b32 a[88:89], v46 offset0:208 offset1:209\n"
    "v_mfma_f32_16x16x32_bf16 v[124:127], a[120:123], a[36:39], v[124:127]\n"
    "ds_read2_b32 a[90:91], v46 offset0:210 offset1:211\n"
    "ds_read_b128 a[28:31], v19 offset:3904\n"
    "s_waitcnt lgkmcnt(15)\n"
    "v_mfma_f32_16x16x32_bf16 v[124:127], a[96:99], a[60:63], v[124:127]\n"
    "ds_read2_b32 a[92:93], v46 offset0:80 offset1:81\n"
    "v_mfma_f32_16x16x32_bf16 v[124:127], a[124:127], a[32:35], v[124:127]\n"
    "ds_read2_b32 a[94:95], v46 offset0:82 offset1:83\n"
    "v_mfma_f32_16x16x32_bf16 v[112:115], a[64:67], a[0:3], v[112:115]\n"
    "s_waitcnt lgkmcnt(15)\n"
    "v_mfma_f32_16x16x32_bf16 v[116:119], a[68:71], a[8:11], v[116:119]\n"
    "ds_read_b128 a[32:35], v19 offset:256\n"
    "ds_read2_b32 a[96:97], v20 offset0:192 offset1:193\n"
    "v_mfma_f32_16x16x32_bf16 v[120:123], a[72:75], a[8:11], v[120:123]\n"
    "ds_read2_b32 a[98:99], v20 offset0:194 offset1:195\n"
    "s_waitcnt lgkmcnt(12)\n"
    "v_mfma_f32_16x16x32_bf16 v[124:127], a[76:79], a[16:19], v[124:127]\n"
    "ds_read_b128 a[36:39], v19 offset:768\n"
    "ds_read2_b32 a[100:101], v20 offset0:64 offset1:65\n"
    "v_mfma_f32_16x16x32_bf16 v[116:119], a[72:75], a[4:7], v[116:119]\n"
    "ds_read2_b32 a[102:103], v20 offset0:66 offset1:67\n"
    "v_mfma_f32_16x16x32_bf16 v[120:123], a[68:71], a[12:15], v[120:123]\n"
    "ds_read_b128 a[40:43], v19 offset:1280\n"
    "ds_read2_b32 a[104:105], v21 offset0:192 offset1:193\n"
    "v_mfma_f32_16x16x32_bf16 v[124:127], a[80:83], a[12:15], v[124:127]\n"
    "ds_read2_b32 a[106:107], v21 offset0:194 offset1:195\n"
    "v_mfma_f32_16x16x32_bf16 v[116:119], a[64:67], a[12:15], v[116:119]\n"
    "ds_read_b128 a[44:47], v19 offset:1792\n"
    "v_mfma_f32_16x16x32_bf16 v[120:123], a[76:79], a[4:7], v[120:123]\n"
    "ds_read2_b32 a[108:109], v21 offset0:64 offset1:65\n"
    "ds_read2_b32 a[110:111], v21 offset0:66 offset1:67\n"
    "s_waitcnt lgkmcnt(15)\n"
    "v_mfma_f32_16x16x32_bf16 v[124:127], a[72:75], a[20:23], v[124:127]\n"
    "ds_read_b128 a[48:51], v19 offset:2304\n"
    "v_mfma_f32_16x16x32_bf16 v[116:119], a[76:79], a[0:3], v[116:119]\n"
    "ds_read2_b32 a[112:113], v45 offset0:192 offset1:193\n"
    "ds_read2_b32 a[114:115], v45 offset0:194 offset1:195\n"
    "v_mfma_f32_16x16x32_bf16 v[120:123], a[64:67], a[16:19], v[120:123]\n"
    "ds_read_b128 a[52:55], v19 offset:2816\n"
    "v_mfma_f32_16x16x32_bf16 v[124:127], a[84:87], a[8:11], v[124:127]\n"
    "ds_read2_b32 a[116:117], v45 offset0:64 offset1:65\n"
    "v_mfma_f32_16x16x32_bf16 v[120:123], a[80:83], a[0:3], v[120:123]\n"
    "ds_read2_b32 a[118:119], v45 offset0:66 offset1:67\n"
    "ds_read_b128 a[56:59], v19 offset:3328\n"
    "v_mfma_f32_16x16x32_bf16 v[124:127], a[68:71], a[24:27], v[124:127]\n"
    "ds_read2_b32 a[120:121], v46 offset0:192 offset1:193\n"
    "v_mfma_f32_16x16x32_bf16 v[124:127], a[88:91], a[4:7], v[124:127]\n"
    "ds_read2_b32 a[122:123], v46 offset0:194 offset1:195\n"
    "ds_read_b128 a[60:63], v19 offset:3840\n"
    "s_waitcnt lgkmcnt(15)\n"
    "v_mfma_f32_16x16x32_bf16 v[124:127], a[64:67], a[28:31], v[124:127]\n"
    "ds_read2_b32 a[124:125], v46 offset0:64 offset1:65\n"
    "v_mfma_f32_16x16x32_bf16 v[124:127], a[92:95], a[0:3], v[124:127]\n"
    "ds_read2_b32 a[126:127], v46 offset0:66 offset1:67\n"
    "v_mfma_f32_16x16x32_bf16 v[112:115], a[96:99], a[32:35], v[112:115]\n"
    "s_waitcnt lgkmcnt(15)\n"
    "v_mfma_f32_16x16x32_bf16 v[116:119], a[100:103], a[40:43], v[116:119]\n"
    "ds_read_b128 a[0:3], v19 offset:192\n"
    "ds_read2_b32 a[64:65], v20 offset0:176 offset1:177\n"
    "v_mfma_f32_16x16x32_bf16 v[120:123], a[104:107], a[40:43], v[120:123]\n"
    "ds_read2_b32 a[66:67], v20 offset0:178 offset1:179\n"
    "s_waitcnt lgkmcnt(12)\n"
    "v_mfma_f32_16x16x32_bf16 v[124:127], a[108:111], a[48:51], v[124:127]\n"
    "ds_read_b128 a[4:7], v19 offset:704\n"
    "ds_read2_b32 a[68:69], v20 offset0:48 offset1:49\n"
    "v_mfma_f32_16x16x32_bf16 v[116:119], a[104:107], a[36:39], v[116:119]\n"
    "ds_read2_b32 a[70:71], v20 offset0:50 offset1:51\n"
    "v_mfma_f32_16x16x32_bf16 v[120:123], a[100:103], a[44:47], v[120:123]\n"
    "ds_read_b128 a[8:11], v19 offset:1216\n"
    "ds_read2_b32 a[72:73], v21 offset0:176 offset1:177\n"
    "v_mfma_f32_16x16x32_bf16 v[124:127], a[112:115], a[44:47], v[124:127]\n"
    "ds_read2_b32 a[74:75], v21 offset0:178 offset1:179\n"
    "v_mfma_f32_16x16x32_bf16 v[116:119], a[96:99], a[44:47], v[116:119]\n"
    "ds_read_b128 a[12:15], v19 offset:1728\n"
    "v_mfma_f32_16x16x32_bf16 v[120:123], a[108:111], a[36:39], v[120:123]\n"
    "ds_read2_b32 a[76:77], v21 offset0:48 offset1:49\n"
    "ds_read2_b32 a[78:79], v21 offset0:50 offset1:51\n"
    "s_waitcnt lgkmcnt(15)\n"
    "v_mfma_f32_16x16x32_bf16 v[124:127], a[104:107], a[52:55], v[124:127]\n"
    "ds_read_b128 a[16:19], v19 offset:2240\n"
    "v_mfma_f32_16x16x32_bf16 v[116:119], a[108:111], a[32:35], v[116:119]\n"
    "ds_read2_b32 a[80:81], v45 offset0:176 offset1:177\n"
    "ds_read2_b32 a[82:83], v45 offset0:178 offset1:179\n"
    "v_mfma_f32_16x16x32_bf16 v[120:123], a[96:99], a[48:51], v[120:123]\n"
    "ds_read_b128 a[20:23], v19 offset:2752\n"
    "v_mfma_f32_16x16x32_bf16 v[124:127], a[116:119], a[40:43], v[124:127]\n"
    "ds_read2_b32 a[84:85], v45 offset0:48 offset1:49\n"
    "v_mfma_f32_16x16x32_bf16 v[120:123], a[112:115], a[32:35], v[120:123]\n"
    "ds_read2_b32 a[86:87], v45 offset0:50 offset1:51\n"
    "ds_read_b128 a[24:27], v19 offset:3264\n"
    "v_mfma_f32_16x16x32_bf16 v[124:127], a[100:103], a[56:59], v[124:127]\n"
    "ds_read2_b32 a[88:89], v46 offset0:176 offset1:177\n"
    "v_mfma_f32_16x16x32_bf16 v[124:127], a[120:123], a[36:39], v[124:127]\n"
    "ds_read2_b32 a[90:91], v46 offset0:178 offset1:179\n"
    "ds_read_b128 a[28:31], v19 offset:3776\n"
    "s_waitcnt lgkmcnt(15)\n"
    "v_mfma_f32_16x16x32_bf16 v[124:127], a[96:99], a[60:63], v[124:127]\n"
    "ds_read2_b32 a[92:93], v46 offset0:48 offset1:49\n"
    "v_mfma_f32_16x16x32_bf16 v[124:127], a[124:127], a[32:35], v[124:127]\n"
    "ds_read2_b32 a[94:95], v46 offset0:50 offset1:51\n"
    "v_mfma_f32_16x16x32_bf16 v[112:115], a[64:67], a[0:3], v[112:115]\n"
    "s_waitcnt lgkmcnt(15)\n"
    "v_mfma_f32_16x16x32_bf16 v[116:119], a[68:71], a[8:11], v[116:119]\n"
    "ds_read_b128 a[32:35], v19 offset:128\n"
    "ds_read2_b32 a[96:97], v20 offset0:160 offset1:161\n"
    "v_mfma_f32_16x16x32_bf16 v[120:123], a[72:75], a[8:11], v[120:123]\n"
    "ds_read2_b32 a[98:99], v20 offset0:162 offset1:163\n"
    "s_waitcnt lgkmcnt(12)\n"
    "v_mfma_f32_16x16x32_bf16 v[124:127], a[76:79], a[16:19], v[124:127]\n"
    "ds_read_b128 a[36:39], v19 offset:640\n"
    "ds_read2_b32 a[100:101], v20 offset0:32 offset1:33\n"
    "v_mfma_f32_16x16x32_bf16 v[116:119], a[72:75], a[4:7], v[116:119]\n"
    "ds_read2_b32 a[102:103], v20 offset0:34 offset1:35\n"
    "v_mfma_f32_16x16x32_bf16 v[120:123], a[68:71], a[12:15], v[120:123]\n"
    "ds_read_b128 a[40:43], v19 offset:1152\n"
    "ds_read2_b32 a[104:105], v21 offset0:160 offset1:161\n"
    "v_mfma_f32_16x16x32_bf16 v[124:127], a[80:83], a[12:15], v[124:127]\n"
    "ds_read2_b32 a[106:107], v21 offset0:162 offset1:163\n"
    "v_mfma_f32_16x16x32_bf16 v[116:119], a[64:67], a[12:15], v[116:119]\n"
    "ds_read_b128 a[44:47], v19 offset:1664\n"
    "v_mfma_f32_16x16x32_bf16 v[120:123], a[76:79], a[4:7], v[120:123]\n"
    "ds_read2_b32 a[108:109], v21 offset0:32 offset1:33\n"
    "ds_read2_b32 a[110:111], v21 offset0:34 offset1:35\n"
    "s_waitcnt lgkmcnt(15)\n"
    "v_mfma_f32_16x16x32_bf16 v[124:127], a[72:75], a[20:23], v[124:127]\n"
    "ds_read_b128 a[48:51], v19 offset:2176\n"
    "v_mfma_f32_16x16x32_bf16 v[116:119], a[76:79], a[0:3], v[116:119]\n"
    "ds_read2_b32 a[112:113], v45 offset0:160 offset1:161\n"
    "ds_read2_b32 a[114:115], v45 offset0:162 offset1:163\n"
    "v_mfma_f32_16x16x32_bf16 v[120:123], a[64:67], a[16:19], v[120:123]\n"
    "ds_read_b128 a[52:55], v19 offset:2688\n"
    "v_mfma_f32_16x16x32_bf16 v[124:127], a[84:87], a[8:11], v[124:127]\n"
    "ds_read2_b32 a[116:117], v45 offset0:32 offset1:33\n"
    "v_mfma_f32_16x16x32_bf16 v[120:123], a[80:83], a[0:3], v[120:123]\n"
    "ds_read2_b32 a[118:119], v45 offset0:34 offset1:35\n"
    "ds_read_b128 a[56:59], v19 offset:3200\n"
    "v_mfma_f32_16x16x32_bf16 v[124:127], a[68:71], a[24:27], v[124:127]\n"
    "ds_read2_b32 a[120:121], v46 offset0:160 offset1:161\n"
    "v_mfma_f32_16x16x32_bf16 v[124:127], a[88:91], a[4:7], v[124:127]\n"
    "ds_read2_b32 a[122:123], v46 offset0:162 offset1:163\n"
    "ds_read_b128 a[60:63], v19 offset:3712\n"
    "s_waitcnt lgkmcnt(15)\n"
    "v_mfma_f32_16x16x32_bf16 v[124:127], a[64:67], a[28:31], v[124:127]\n"
    "ds_read2_b32 a[124:125], v46 offset0:32 offset1:33\n"
    "v_mfma_f32_16x16x32_bf16 v[124:127], a[92:95], a[0:3], v[124:127]\n"
    "ds_read2_b32 a[126:127], v46 offset0:34 offset1:35\n"
    "v_mfma_f32_16x16x32_bf16 v[112:115], a[96:99], a[32:35], v[112:115]\n"
    "s_waitcnt lgkmcnt(15)\n"
    "v_mfma_f32_16x16x32_bf16 v[116:119], a[100:103], a[40:43], v[116:119]\n"
    "ds_read_b128 a[0:3], v19 offset:64\n"
    "ds_read2_b32 a[64:65], v20 offset0:144 offset1:145\n"
    "v_mfma_f32_16x16x32_bf16 v[120:123], a[104:107], a[40:43], v[120:123]\n"
    "ds_read2_b32 a[66:67], v20 offset0:146 offset1:147\n"
    "s_waitcnt lgkmcnt(12)\n"
    "v_mfma_f32_16x16x32_bf16 v[124:127], a[108:111], a[48:51], v[124:127]\n"
    "ds_read_b128 a[4:7], v19 offset:576\n"
    "ds_read2_b32 a[68:69], v20 offset0:16 offset1:17\n"
    "v_mfma_f32_16x16x32_bf16 v[116:119], a[104:107], a[36:39], v[116:119]\n"
    "ds_read2_b32 a[70:71], v20 offset0:18 offset1:19\n"
    "v_mfma_f32_16x16x32_bf16 v[120:123], a[100:103], a[44:47], v[120:123]\n"
    "ds_read_b128 a[8:11], v19 offset:1088\n"
    "ds_read2_b32 a[72:73], v21 offset0:144 offset1:145\n"
    "v_mfma_f32_16x16x32_bf16 v[124:127], a[112:115], a[44:47], v[124:127]\n"
    "ds_read2_b32 a[74:75], v21 offset0:146 offset1:147\n"
    "v_mfma_f32_16x16x32_bf16 v[116:119], a[96:99], a[44:47], v[116:119]\n"
    "ds_read_b128 a[12:15], v19 offset:1600\n"
    "v_mfma_f32_16x16x32_bf16 v[120:123], a[108:111], a[36:39], v[120:123]\n"
    "ds_read2_b32 a[76:77], v21 offset0:16 offset1:17\n"
    "ds_read2_b32 a[78:79], v21 offset0:18 offset1:19\n"
    "s_waitcnt lgkmcnt(15)\n"
    "v_mfma_f32_16x16x32_bf16 v[124:127], a[104:107], a[52:55], v[124:127]\n"
    "ds_read_b128 a[16:19], v19 offset:2112\n"
    "v_mfma_f32_16x16x32_bf16 v[116:119], a[108:111], a[32:35], v[116:119]\n"
    "ds_read2_b32 a[80:81], v45 offset0:144 offset1:145\n"
    "ds_read2_b32 a[82:83], v45 offset0:146 offset1:147\n"
    "v_mfma_f32_16x16x32_bf16 v[120:123], a[96:99], a[48:51], v[120:123]\n"
    "ds_read_b128 a[20:23], v19 offset:2624\n"
    "v_mfma_f32_16x16x32_bf16 v[124:127], a[116:119], a[40:43], v[124:127]\n"
    "ds_read2_b32 a[84:85], v45 offset0:16 offset1:17\n"
    "v_mfma_f32_16x16x32_bf16 v[120:123], a[112:115], a[32:35], v[120:123]\n"
    "ds_read2_b32 a[86:87], v45 offset0:18 offset1:19\n"
    "ds_read_b128 a[24:27], v19 offset:3136\n"
    "v_mfma_f32_16x16x32_bf16 v[124:127], a[100:103], a[56:59], v[124:127]\n"
    "ds_read2_b32 a[88:89], v46 offset0:144 offset1:145\n"
    "v_mfma_f32_16x16x32_bf16 v[124:127], a[120:123], a[36:39], v[124:127]\n"
    "ds_read2_b32 a[90:91], v46 offset0:146 offset1:147\n"
    "ds_read_b128 a[28:31], v19 offset:3648\n"
    "s_waitcnt lgkmcnt(15)\n"
    "v_mfma_f32_16x16x32_bf16 v[124:127], a[96:99], a[60:63], v[124:127]\n"
    "ds_read2_b32 a[92:93], v46 offset0:16 offset1:17\n"
    "v_mfma_f32_16x16x32_bf16 v[124:127], a[124:127], a[32:35], v[124:127]\n"
    "ds_read2_b32 a[94:95], v46 offset0:18 offset1:19\n"
    "v_mfma_f32_16x16x32_bf16 v[112:115], a[64:67], a[0:3], v[112:115]\n"
    "s_waitcnt lgkmcnt(15)\n"
    "v_mfma_f32_16x16x32_bf16 v[116:119], a[68:71], a[8:11], v[116:119]\n"
    "ds_read_b128 a[32:35], v19 offset:0\n"
    "ds_read2_b32 a[96:97], v20 offset0:128 offset1:129\n"
    "v_mfma_f32_16x16x32_bf16 v[120:123], a[72:75], a[8:11], v[120:123]\n"
    "ds_read2_b32 a[98:99], v20 offset0:130 offset1:131\n"
    "s_waitcnt lgkmcnt(12)\n"
    "v_mfma_f32_16x16x32_bf16 v[124:127], a[76:79], a[16:19], v[124:127]\n"
    "ds_read_b128 a[36:39], v19 offset:512\n"
    "ds_read2_b32 a[100:101], v20 offset0:0 offset1:1\n"
    "v_mfma_f32_16x16x32_bf16 v[116:119], a[72:75], a[4:7], v[116:119]\n"
    "ds_read2_b32 a[102:103], v20 offset0:2 offset1:3\n"
    "v_mfma_f32_16x16x32_bf16 v[120:123], a[68:71], a[12:15], v[120:123]\n"
    "ds_read_b128 a[40:43], v19 offset:1024\n"
    "ds_read2_b32 a[104:105], v21 offset0:128 offset1:129\n"
    "v_mfma_f32_16x16x32_bf16 v[124:127], a[80:83], a[12:15], v[124:127]\n"
    "ds_read2_b32 a[106:107], v21 offset0:130 offset1:131\n"
    "v_mfma_f32_16x16x32_bf16 v[116:119], a[64:67], a[12:15], v[116:119]\n"
    "ds_read_b128 a[44:47], v19 offset:1536\n"
    "v_mfma_f32_16x16x32_bf16 v[120:123], a[76:79], a[4:7], v[120:123]\n"
    "ds_read2_b32 a[108:109], v21 offset0:0 offset1:1\n"
    "ds_read2_b32 a[110:111], v21 offset0:2 offset1:3\n"
    "s_waitcnt lgkmcnt(15)\n"
    "v_mfma_f32_16x16x32_bf16 v[124:127], a[72:75], a[20:23], v[124:127]\n"
    "ds_read_b128 a[48:51], v19 offset:2048\n"
    "v_mfma_f32_16x16x32_bf16 v[116:119], a[76:79], a[0:3], v[116:119]\n"
    "ds_read2_b32 a[112:113], v45 offset0:128 offset1:129\n"
    "ds_read2_b32 a[114:115], v45 offset0:130 offset1:131\n"
    "v_mfma_f32_16x16x32_bf16 v[120:123], a[64:67], a[16:19], v[120:123]\n"
    "ds_read_b128 a[52:55], v19 offset:2560\n"
    "v_mfma_f32_16x16x32_bf16 v[124:127], a[84:87], a[8:11], v[124:127]\n"
    "ds_read2_b32 a[116:117], v45 offset0:0 offset1:1\n"
    "v_mfma_f32_16x16x32_bf16 v[120:123], a[80:83], a[0:3], v[120:123]\n"
    "ds_read2_b32 a[118:119], v45 offset0:2 offset1:3\n"
    "ds_read_b128 a[56:59], v19 offset:3072\n"
    "v_mfma_f32_16x16x32_bf16 v[124:127], a[68:71], a[24:27], v[124:127]\n"
    "ds_read2_b32 a[120:121], v46 offset0:128 offset1:129\n"
    "v_mfma_f32_16x16x32_bf16 v[124:127], a[88:91], a[4:7], v[124:127]\n"
    "ds_read2_b32 a[122:123], v46 offset0:130 offset1:131\n"
    "ds_read_b128 a[60:63], v19 offset:3584\n"
    "s_waitcnt lgkmcnt(15)\n"
    "v_mfma_f32_16x16x32_bf16 v[124:127], a[64:67], a[28:31], v[124:127]\n"
    "ds_read2_b32 a[124:125], v46 offset0:0 offset1:1\n"
    "v_mfma_f32_16x16x32_bf16 v[124:127], a[92:95], a[0:3], v[124:127]\n"
    "ds_read2_b32 a[126:127], v46 offset0:2 offset1:3\n"
    "v_mfma_f32_16x16x32_bf16 v[112:115], a[96:99], a[32:35], v[112:115]\n"
    "s_waitcnt lgkmcnt(15)\n"
    "v_mfma_f32_16x16x32_bf16 v[116:119], a[100:103], a[40:43], v[116:119]\n"
    "v_mfma_f32_16x16x32_bf16 v[120:123], a[104:107], a[40:43], v[120:123]\n"
    "s_waitcnt lgkmcnt(9)\n"
    "v_mfma_f32_16x16x32_bf16 v[124:127], a[108:111], a[48:51], v[124:127]\n"
    "v_mfma_f32_16x16x32_bf16 v[116:119], a[104:107], a[36:39], v[116:119]\n"
    "v_mfma_f32_16x16x32_bf16 v[120:123], a[100:103], a[44:47], v[120:123]\n"
    "v_mfma_f32_16x16x32_bf16 v[124:127], a[112:115], a[44:47], v[124:127]\n"
    "v_mfma_f32_16x16x32_bf16 v[116:119], a[96:99], a[44:47], v[116:119]\n"
    "v_mfma_f32_16x16x32_bf16 v[120:123], a[108:111], a[36:39], v[120:123]\n"
    "s_waitcnt lgkmcnt(6)\n"
    "v_mfma_f32_16x16x32_bf16 v[124:127], a[104:107], a[52:55], v[124:127]\n"
    "v_mfma_f32_16x16x32_bf16 v[116:119], a[108:111], a[32:35], v[116:119]\n"
    "v_mfma_f32_16x16x32_bf16 v[120:123], a[96:99], a[48:51], v[120:123]\n"
    "v_mfma_f32_16x16x32_bf16 v[124:127], a[116:119], a[40:43], v[124:127]\n"
    "v_mfma_f32_16x16x32_bf16 v[120:123], a[112:115], a[32:35], v[120:123]\n"
    "s_waitcnt lgkmcnt(3)\n"
    "v_mfma_f32_16x16x32_bf16 v[124:127], a[100:103], a[56:59], v[124:127]\n"
    "v_mfma_f32_16x16x32_bf16 v[124:127], a[120:123], a[36:39], v[124:127]\n"
    "s_waitcnt lgkmcnt(0)\n"
    "v_mfma_f32_16x16x32_bf16 v[124:127], a[96:99], a[60:63], v[124:127]\n"
    "v_mfma_f32_16x16x32_bf16 v[124:127], a[124:127], a[32:35], v[124:127]\n"
    ".Leven_%=:\n"
    "s_setprio 2\n"
    "v_cvt_pk_bf16_f32 v104, v112, v113\n"
    "v_cvt_pk_bf16_f32 v105, v114, v115\n"
    "v_cvt_pk_bf16_f32 v106, v116, v117\n"
    "v_cvt_pk_bf16_f32 v107, v118, v119\n"
    "s_nop 0\n"
    "v_permlane16_swap_b32 v104, v106\n"
    "v_permlane16_swap_b32 v105, v107\n"
    "buffer_store_dwordx4 v[104:107], v16, s[24:27], s45 offen offset:0 sc1\n"
    "v_cvt_pk_bf16_f32 v108, v120, v121\n"
    "v_cvt_pk_bf16_f32 v109, v122, v123\n"
    "v_cvt_pk_bf16_f32 v110, v124, v125\n"
    "v_cvt_pk_bf16_f32 v111, v126, v127\n"
    "s_nop 0\n"
    "v_permlane16_swap_b32 v108, v110\n"
    "v_permlane16_swap_b32 v109, v111\n"
    "buffer_store_dwordx4 v[108:111], v16, s[24:27], s45 offen offset:2048 sc1\n"
    "s_lshr_b32 s12, s3, 1\n"
    "s_and_b32 s13, s3, 1\n"
    "s_lshl_b32 s14, s7, 1\n"
    "s_add_u32 s14, s14, s12\n"
    "s_lshl_b32 s15, s8, 1\n"
    "s_add_u32 s15, s15, s13\n"
    "s_lshl_b32 s12, s15, 6\n"
    "s_load_dwordx16 s[64:79], s[72:73], s12\n"
    "v_bfe_u32 v17, v1, 2, 2\n"
    "v_and_b32 v18, 3, v1\n"
    "v_bfe_u32 v11, v1, 4, 1\n"
    "s_lshl_b32 s12, s14, 7\n"
    "s_add_u32 s12, s12, 16384\n"
    "v_lshl_add_u32 v19, v2, 2, s12\n"
    "s_lshl_b32 s12, s15, 17\n"
    "s_lshl_b32 s13, s14, 6\n"
    "s_add_u32 s39, s12, s13\n"
    "s_add_u32 s39, s39, 2260992\n"
    "s_add_u32 s40, s39, 65536\n"
    "v_lshrrev_b32 v8, 2, v1\n"
    "v_lshlrev_b32 v9, 4, v18\n"
    "v_lshl_or_b32 v20, v8, 12, v9\n"
    "s_lshl_b32 s12, s3, 12\n"
    "s_add_u32 s12, s12, 64384\n"
    "v_mul_u32_u24 v8, 80, v8\n"
    "v_add3_u32 v21, v8, v9, s12\n"
    "v_lshlrev_b32 v9, 3, v18\n"
    "v_lshl_or_b32 v9, v11, 5, v9\n"
    "v_lshl_add_u32 v8, v3, 2, v17\n"
    "v_mul_u32_u24 v8, 80, v8\n"
    "v_add3_u32 v25, v8, v9, s12\n"
    "s_mul_i32 s12, s3, 4352\n"
    "s_add_u32 s12, s12, 25984\n"
    "v_add_u32 v30, s12, v6\n"
    "s_waitcnt vmcnt(0)\n"
    "s_mov_b64 exec, 1\n"
    "buffer_store_dword v12, off, s[24:27], s62 sc1\n"
    "s_mov_b64 exec, -1\n"
    "s_or_b32 s12, s2, s3\n"
    "s_cmp_lg_u32 s12, 0\n"
    "s_cbranch_scc0 .Lep_%=\n"
    ".Lep_done_%=:\n"
    "s_waitcnt lgkmcnt(0)\n"
    "v_cmp_ne_u32 vcc, 0, v3\n"
    "v_mov_b32 v8, s64\n"
    "v_mov_b32 v9, s66\n"
    "v_cndmask_b32 v8, v8, v9, vcc\n"
    "v_lshlrev_b32 v72, 16, v8\n"
    "v_and_b32 v73, 0xffff0000, v8\n"
    "v_mov_b32 v8, s65\n"
    "v_mov_b32 v9, s67\n"
    "v_cndmask_b32 v8, v8, v9, vcc\n"
    "v_lshlrev_b32 v74, 16, v8\n"
    "v_and_b32 v75, 0xffff0000, v8\n"
    "v_mov_b32 v8, s68\n"
    "v_mov_b32 v9, s70\n"
    "v_cndmask_b32 v8, v8, v9, vcc\n"
    "v_lshlrev_b32 v76, 16, v8\n"
    "v_and_b32 v77, 0xffff0000, v8\n"
    "v_mov_b32 v8, s69\n"
    "v_mov_b32 v9, s71\n"
    "v_cndmask_b32 v8, v8, v9, vcc\n"
    "v_lshlrev_b32 v78, 16, v8\n"
    "v_and_b32 v79, 0xffff0000, v8\n"
    "v_mov_b32 v8, s72\n"
    "v_mov_b32 v9, s74\n"
    "v_cndmask_b32 v8, v8, v9, vcc\n"
    "v_lshlrev_b32 v80, 16, v8\n"
    "v_and_b32 v81, 0xffff0000, v8\n"
    "v_mov_b32 v8, s73\n"
    "v_mov_b32 v9, s75\n"
    "v_cndmask_b32 v8, v8, v9, vcc\n"
    "v_lshlrev_b32 v82, 16, v8\n"
    "v_and_b32 v83, 0xffff0000, v8\n"
    "v_mov_b32 v8, s76\n"
    "v_mov_b32 v9, s78\n"
    "v_cndmask_b32 v8, v8, v9, vcc\n"
    "v_lshlrev_b32 v84, 16, v8\n"
    "v_and_b32 v85, 0xffff0000, v8\n"
    "v_mov_b32 v8, s77\n"
    "v_mov_b32 v9, s79\n"
    "v_cndmask_b32 v8, v8, v9, vcc\n"
    "v_lshlrev_b32 v86, 16, v8\n"
    "v_and_b32 v87, 0xffff0000, v8\n"
    "v_mov_b32 v22, v23\n"
    "ds_read_b32 v24, v22 offset:0\n"
    "s_waitcnt lgkmcnt(0)\n"
    "v_cmp_gt_u32 vcc, 1, v24\n"
    "s_nop 1\n"
    "s_cbranch_vccnz .Lyg_slow_%=\n"
    ".Lyg_ok_%=:\n"
    "buffer_load_dword v14, v13, s[24:27], 0 offen sc1\n"
    "s_waitcnt vmcnt(0)\n"
    "v_cmp_ne_u32 vcc, s80, v14\n"
    "s_nop 1\n"
    "s_cbranch_vccnz .Lfy_slow_%=\n"
    ".Lfy_ok_%=:\n"
    "buffer_load_dword v19, v19, s[24:27], 0 offen sc1\n"
    "buffer_load_dwordx4 v[96:99], v20, s[24:27], s39 offen sc1\n"
    "buffer_load_dwordx4 v[100:103], v20, s[24:27], s40 offen sc1\n"
    "v_lshrrev_b32 v8, 2, v6\n"
    "v_sub_u32 v9, v30, v6\n"
    "v_add_u32 v8, v8, v9\n"
    "s_waitcnt vmcnt(2)\n"
    "v_sub_f32 v31, 1.0, v19\n"
    "v_cvt_pk_bf16_f32 v31, v31, v31\n"
    "v_lshlrev_b32 v31, 16, v31\n"
    "v_mul_f32 v72, v31, v72\n"
    "v_mul_f32 v73, v31, v73\n"
    "v_mul_f32 v74, v31, v74\n"
    "v_mul_f32 v75, v31, v75\n"
    "v_mul_f32 v76, v31, v76\n"
    "v_mul_f32 v77, v31, v77\n"
    "v_mul_f32 v78, v31, v78\n"
    "v_mul_f32 v79, v31, v79\n"
    "v_mul_f32 v80, v31, v80\n"
    "v_mul_f32 v81, v31, v81\n"
    "v_mul_f32 v82, v31, v82\n"
    "v_mul_f32 v83, v31, v83\n"
    "v_mul_f32 v84, v31, v84\n"
    "v_mul_f32 v85, v31, v85\n"
    "v_mul_f32 v86, v31, v86\n"
    "v_mul_f32 v87, v31, v87\n"
    "ds_write_b32 v8, v31 offset:4096\n"
    "s_waitcnt vmcnt(0)\n"
    "ds_write_b128 v21, v[96:99] offset:0\n"
    "ds_write_b128 v21, v[100:103] offset:1280\n"
    "ds_read_b64_tr_b16 v[64:65], v25 offset:0\n"
    "ds_read_b64_tr_b16 v[66:67], v25 offset:640\n"
    "ds_read_b64_tr_b16 v[68:69], v25 offset:1280\n"
    "ds_read_b64_tr_b16 v[70:71], v25 offset:1920\n"
    "s_waitcnt lgkmcnt(3)\n"
    "v_lshlrev_b32 v8, 16, v64\n"
    "v_and_b32 v9, 0xffff0000, v64\n"
    "v_fmac_f32 v72, v19, v8\n"
    "v_fmac_f32 v73, v19, v9\n"
    "v_lshlrev_b32 v8, 16, v65\n"
    "v_and_b32 v9, 0xffff0000, v65\n"
    "v_fmac_f32 v74, v19, v8\n"
    "v_fmac_f32 v75, v19, v9\n"
    "ds_write_b128 v30, v[72:75] offset:0\n"
    "s_waitcnt lgkmcnt(3)\n"
    "v_lshlrev_b32 v8, 16, v66\n"
    "v_and_b32 v9, 0xffff0000, v66\n"
    "v_fmac_f32 v76, v19, v8\n"
    "v_fmac_f32 v77, v19, v9\n"
    "v_lshlrev_b32 v8, 16, v67\n"
    "v_and_b32 v9, 0xffff0000, v67\n"
    "v_fmac_f32 v78, v19, v8\n"
    "v_fmac_f32 v79, v19, v9\n"
    "ds_write_b128 v30, v[76:79] offset:1024\n"
    "s_waitcnt lgkmcnt(3)\n"
    "v_lshlrev_b32 v8, 16, v68\n"
    "v_and_b32 v9, 0xffff0000, v68\n"
    "v_fmac_f32 v80, v19, v8\n"
    "v_fmac_f32 v81, v19, v9\n"
    "v_lshlrev_b32 v8, 16, v69\n"
    "v_and_b32 v9, 0xffff0000, v69\n"
    "v_fmac_f32 v82, v19, v8\n"
    "v_fmac_f32 v83, v19, v9\n"
    "ds_write_b128 v30, v[80:83] offset:2048\n"
    "s_waitcnt lgkmcnt(3)\n"
    "v_lshlrev_b32 v8, 16, v70\n"
    "v_and_b32 v9, 0xffff0000, v70\n"
    "v_fmac_f32 v84, v19, v8\n"
    "v_fmac_f32 v85, v19, v9\n"
    "v_lshlrev_b32 v8, 16, v71\n"
    "v_and_b32 v9, 0xffff0000, v71\n"
    "v_fmac_f32 v86, v19, v8\n"
    "v_fmac_f32 v87, v19, v9\n"
    "ds_write_b128 v30, v[84:87] offset:3072\n"
    "s_waitcnt lgkmcnt(0)\n"
    "s_barrier\n"
    "s_endpgm\n"
    ".Lodd_%=:\n"
    "ds_read_b128 a[0:3], v19 offset:448\n"
    "ds_read2_b32 a[64:65], v20 offset0:240 offset1:241\n"
    "ds_read2_b32 a[66:67], v20 offset0:242 offset1:243\n"
    "ds_read_b128 a[4:7], v19 offset:960\n"
    "ds_read2_b32 a[68:69], v20 offset0:112 offset1:113\n"
    "ds_read2_b32 a[70:71], v20 offset0:114 offset1:115\n"
    "ds_read_b128 a[8:11], v19 offset:1472\n"
    "ds_read2_b32 a[72:73], v21 offset0:240 offset1:241\n"
    "ds_read2_b32 a[74:75], v21 offset0:242 offset1:243\n"
    "ds_read_b128 a[12:15], v19 offset:1984\n"
    "ds_read2_b32 a[76:77], v21 offset0:112 offset1:113\n"
    "ds_read2_b32 a[78:79], v21 offset0:114 offset1:115\n"
    "ds_read_b128 a[16:19], v19 offset:2496\n"
    "ds_read2_b32 a[80:81], v45 offset0:240 offset1:241\n"
    "ds_read2_b32 a[82:83], v45 offset0:242 offset1:243\n"
    "ds_read_b128 a[20:23], v19 offset:3008\n"
    "ds_read2_b32 a[84:85], v45 offset0:112 offset1:113\n"
    "ds_read2_b32 a[86:87], v45 offset0:114 offset1:115\n"
    "ds_read_b128 a[24:27], v19 offset:3520\n"
    "ds_read2_b32 a[88:89], v46 offset0:240 offset1:241\n"
    "ds_read2_b32 a[90:91], v46 offset0:242 offset1:243\n"
    "s_waitcnt lgkmcnt(15)\n"
    "v_mfma_f32_16x16x32_bf16 v[112:115], a[64:67], a[4:7], 0\n"
    "v_mfma_f32_16x16x32_bf16 v[116:119], a[68:71], a[4:7], 0\n"
    "ds_read_b128 a[32:35], v19 offset:384\n"
    "ds_read2_b32 a[96:97], v20 offset0:224 offset1:225\n"
    "s_waitcnt lgkmcnt(11)\n"
    "v_mfma_f32_16x16x32_bf16 v[120:123], a[72:75], a[12:15], 0\n"
    "ds_read2_b32 a[98:99], v20 offset0:226 offset1:227\n"
    "v_mfma_f32_16x16x32_bf16 v[112:115], a[68:71], a[0:3], v[112:115]\n"
    "ds_read_b128 a[36:39], v19 offset:896\n"
    "v_mfma_f32_16x16x32_bf16 v[116:119], a[64:67], a[8:11], v[116:119]\n"
    "ds_read2_b32 a[100:101], v20 offset0:96 offset1:97\n"
    "v_mfma_f32_16x16x32_bf16 v[120:123], a[76:79], a[8:11], v[120:123]\n"
    "ds_read2_b32 a[102:103], v20 offset0:98 offset1:99\n"
    "ds_read_b128 a[40:43], v19 offset:1408\n"
    "v_mfma_f32_16x16x32_bf16 v[124:127], a[76:79], a[12:15], 0\n"
    "ds_read2_b32 a[104:105], v21 offset0:224 offset1:225\n"
    "v_mfma_f32_16x16x32_bf16 v[116:119], a[72:75], a[0:3], v[116:119]\n"
    "ds_read2_b32 a[106:107], v21 offset0:226 offset1:227\n"
    "s_waitcnt lgkmcnt(15)\n"
    "v_mfma_f32_16x16x32_bf16 v[120:123], a[68:71], a[16:19], v[120:123]\n"
    "ds_read_b128 a[44:47], v19 offset:1920\n"
    "v_mfma_f32_16x16x32_bf16 v[124:127], a[72:75], a[16:19], v[124:127]\n"
    "ds_read2_b32 a[108:109], v21 offset0:96 offset1:97\n"
    "ds_read2_b32 a[110:111], v21 offset0:98 offset1:99\n"
    "v_mfma_f32_16x16x32_bf16 v[120:123], a[80:83], a[4:7], v[120:123]\n"
    "ds_read_b128 a[48:51], v19 offset:2432\n"
    "v_mfma_f32_16x16x32_bf16 v[124:127], a[80:83], a[8:11], v[124:127]\n"
    "ds_read2_b32 a[112:113], v45 offset0:224 offset1:225\n"
    "s_waitcnt lgkmcnt(15)\n"
    "v_mfma_f32_16x16x32_bf16 v[120:123], a[64:67], a[20:23], v[120:123]\n"
    "ds_read2_b32 a[114:115], v45 offset0:226 offset1:227\n"
    "v_mfma_f32_16x16x32_bf16 v[120:123], a[84:87], a[0:3], v[120:123]\n"
    "ds_read_b128 a[52:55], v19 offset:2944\n"
    "ds_read2_b32 a[116:117], v45 offset0:96 offset1:97\n"
    "v_mfma_f32_16x16x32_bf16 v[124:127], a[68:71], a[20:23], v[124:127]\n"
    "ds_read2_b32 a[118:119], v45 offset0:98 offset1:99\n"
    "v_mfma_f32_16x16x32_bf16 v[124:127], a[84:87], a[4:7], v[124:127]\n"
    "ds_read_b128 a[56:59], v19 offset:3456\n"
    "s_waitcnt lgkmcnt(15)\n"
    "v_mfma_f32_16x16x32_bf16 v[124:127], a[64:67], a[24:27], v[124:127]\n"
    "ds_read2_b32 a[120:121], v46 offset0:224 offset1:225\n"
    "v_mfma_f32_16x16x32_bf16 v[124:127], a[88:91], a[0:3], v[124:127]\n"
    "ds_read2_b32 a[122:123], v46 offset0:226 offset1:227\n"
    "s_waitcnt lgkmcnt(15)\n"
    "v_mfma_f32_16x16x32_bf16 v[112:115], a[96:99], a[36:39], v[112:115]\n"
    "v_mfma_f32_16x16x32_bf16 v[116:119], a[100:103], a[36:39], v[116:119]\n"
    "ds_read_b128 a[0:3], v19 offset:320\n"
    "ds_read2_b32 a[64:65], v20 offset0:208 offset1:209\n"
    "s_waitcnt lgkmcnt(11)\n"
    "v_mfma_f32_16x16x32_bf16 v[120:123], a[104:107], a[44:47], v[120:123]\n"
    "ds_read2_b32 a[66:67], v20 offset0:210 offset1:211\n"
    "v_mfma_f32_16x16x32_bf16 v[112:115], a[100:103], a[32:35], v[112:115]\n"
    "ds_read_b128 a[4:7], v19 offset:832\n"
    "v_mfma_f32_16x16x32_bf16 v[116:119], a[96:99], a[40:43], v[116:119]\n"
    "ds_read2_b32 a[68:69], v20 offset0:80 offset1:81\n"
    "v_mfma_f32_16x16x32_bf16 v[120:123], a[108:111], a[40:43], v[120:123]\n"
    "ds_read2_b32 a[70:71], v20 offset0:82 offset1:83\n"
    "ds_read_b128 a[8:11], v19 offset:1344\n"
    "v_mfma_f32_16x16x32_bf16 v[124:127], a[108:111], a[44:47], v[124:127]\n"
    "ds_read2_b32 a[72:73], v21 offset0:208 offset1:209\n"
    "v_mfma_f32_16x16x32_bf16 v[116:119], a[104:107], a[32:35], v[116:119]\n"
    "ds_read2_b32 a[74:75], v21 offset0:210 offset1:211\n"
    "s_waitcnt lgkmcnt(15)\n"
    "v_mfma_f32_16x16x32_bf16 v[120:123], a[100:103], a[48:51], v[120:123]\n"
    "ds_read_b128 a[12:15], v19 offset:1856\n"
    "v_mfma_f32_16x16x32_bf16 v[124:127], a[104:107], a[48:51], v[124:127]\n"
    "ds_read2_b32 a[76:77], v21 offset0:80 offset1:81\n"
    "ds_read2_b32 a[78:79], v21 offset0:82 offset1:83\n"
    "v_mfma_f32_16x16x32_bf16 v[120:123], a[112:115], a[36:39], v[120:123]\n"
    "ds_read_b128 a[16:19], v19 offset:2368\n"
    "v_mfma_f32_16x16x32_bf16 v[124:127], a[112:115], a[40:43], v[124:127]\n"
    "ds_read2_b32 a[80:81], v45 offset0:208 offset1:209\n"
    "s_waitcnt lgkmcnt(15)\n"
    "v_mfma_f32_16x16x32_bf16 v[120:123], a[96:99], a[52:55], v[120:123]\n"
    "ds_read2_b32 a[82:83], v45 offset0:210 offset1:211\n"
    "v_mfma_f32_16x16x32_bf16 v[120:123], a[116:119], a[32:35], v[120:123]\n"
    "ds_read_b128 a[20:23], v19 offset:2880\n"
    "ds_read2_b32 a[84:85], v45 offset0:80 offset1:81\n"
    "v_mfma_f32_16x16x32_bf16 v[124:127], a[100:103], a[52:55], v[124:127]\n"
    "ds_read2_b32 a[86:87], v45 offset0:82 offset1:83\n"
    "v_mfma_f32_16x16x32_bf16 v[124:127], a[116:119], a[36:39], v[124:127]\n"
    "ds_read_b128 a[24:27], v19 offset:3392\n"
    "s_waitcnt lgkmcnt(15)\n"
    "v_mfma_f32_16x16x32_bf16 v[124:127], a[96:99], a[56:59], v[124:127]\n"
    "ds_read2_b32 a[88:89], v46 offset0:208 offset1:209\n"
    "v_mfma_f32_16x16x32_bf16 v[124:127], a[120:123], a[32:35], v[124:127]\n"
    "ds_read2_b32 a[90:91], v46 offset0:210 offset1:211\n"
    "s_waitcnt lgkmcnt(15)\n"
    "v_mfma_f32_16x16x32_bf16 v[112:115], a[64:67], a[4:7], v[112:115]\n"
    "v_mfma_f32_16x16x32_bf16 v[116:119], a[68:71], a[4:7], v[116:119]\n"
    "ds_read_b128 a[32:35], v19 offset:256\n"
    "ds_read2_b32 a[96:97], v20 offset0:192 offset1:193\n"
    "s_waitcnt lgkmcnt(11)\n"
    "v_mfma_f32_16x16x32_bf16 v[120:123], a[72:75], a[12:15], v[120:123]\n"
    "ds_read2_b32 a[98:99], v20 offset0:194 offset1:195\n"
    "v_mfma_f32_16x16x32_bf16 v[112:115], a[68:71], a[0:3], v[112:115]\n"
    "ds_read_b128 a[36:39], v19 offset:768\n"
    "v_mfma_f32_16x16x32_bf16 v[116:119], a[64:67], a[8:11], v[116:119]\n"
    "ds_read2_b32 a[100:101], v20 offset0:64 offset1:65\n"
    "v_mfma_f32_16x16x32_bf16 v[120:123], a[76:79], a[8:11], v[120:123]\n"
    "ds_read2_b32 a[102:103], v20 offset0:66 offset1:67\n"
    "ds_read_b128 a[40:43], v19 offset:1280\n"
    "v_mfma_f32_16x16x32_bf16 v[124:127], a[76:79], a[12:15], v[124:127]\n"
    "ds_read2_b32 a[104:105], v21 offset0:192 offset1:193\n"
    "v_mfma_f32_16x16x32_bf16 v[116:119], a[72:75], a[0:3], v[116:119]\n"
    "ds_read2_b32 a[106:107], v21 offset0:194 offset1:195\n"
    "s_waitcnt lgkmcnt(15)\n"
    "v_mfma_f32_16x16x32_bf16 v[120:123], a[68:71], a[16:19], v[120:123]\n"
    "ds_read_b128 a[44:47], v19 offset:1792\n"
    "v_mfma_f32_16x16x32_bf16 v[124:127], a[72:75], a[16:19], v[124:127]\n"
    "ds_read2_b32 a[108:109], v21 offset0:64 offset1:65\n"
    "ds_read2_b32 a[110:111], v21 offset0:66 offset1:67\n"
    "v_mfma_f32_16x16x32_bf16 v[120:123], a[80:83], a[4:7], v[120:123]\n"
    "ds_read_b128 a[48:51], v19 offset:2304\n"
    "v_mfma_f32_16x16x32_bf16 v[124:127], a[80:83], a[8:11], v[124:127]\n"
    "ds_read2_b32 a[112:113], v45 offset0:192 offset1:193\n"
    "s_waitcnt lgkmcnt(15)\n"
    "v_mfma_f32_16x16x32_bf16 v[120:123], a[64:67], a[20:23], v[120:123]\n"
    "ds_read2_b32 a[114:115], v45 offset0:194 offset1:195\n"
    "v_mfma_f32_16x16x32_bf16 v[120:123], a[84:87], a[0:3], v[120:123]\n"
    "ds_read_b128 a[52:55], v19 offset:2816\n"
    "ds_read2_b32 a[116:117], v45 offset0:64 offset1:65\n"
    "v_mfma_f32_16x16x32_bf16 v[124:127], a[68:71], a[20:23], v[124:127]\n"
    "ds_read2_b32 a[118:119], v45 offset0:66 offset1:67\n"
    "v_mfma_f32_16x16x32_bf16 v[124:127], a[84:87], a[4:7], v[124:127]\n"
    "ds_read_b128 a[56:59], v19 offset:3328\n"
    "s_waitcnt lgkmcnt(15)\n"
    "v_mfma_f32_16x16x32_bf16 v[124:127], a[64:67], a[24:27], v[124:127]\n"
    "ds_read2_b32 a[120:121], v46 offset0:192 offset1:193\n"
    "v_mfma_f32_16x16x32_bf16 v[124:127], a[88:91], a[0:3], v[124:127]\n"
    "ds_read2_b32 a[122:123], v46 offset0:194 offset1:195\n"
    "s_waitcnt lgkmcnt(15)\n"
    "v_mfma_f32_16x16x32_bf16 v[112:115], a[96:99], a[36:39], v[112:115]\n"
    "v_mfma_f32_16x16x32_bf16 v[116:119], a[100:103], a[36:39], v[116:119]\n"
    "ds_read_b128 a[0:3], v19 offset:192\n"
    "ds_read2_b32 a[64:65], v20 offset0:176 offset1:177\n"
    "s_waitcnt lgkmcnt(11)\n"
    "v_mfma_f32_16x16x32_bf16 v[120:123], a[104:107], a[44:47], v[120:123]\n"
    "ds_read2_b32 a[66:67], v20 offset0:178 offset1:179\n"
    "v_mfma_f32_16x16x32_bf16 v[112:115], a[100:103], a[32:35], v[112:115]\n"
    "ds_read_b128 a[4:7], v19 offset:704\n"
    "v_mfma_f32_16x16x32_bf16 v[116:119], a[96:99], a[40:43], v[116:119]\n"
    "ds_read2_b32 a[68:69], v20 offset0:48 offset1:49\n"
    "v_mfma_f32_16x16x32_bf16 v[120:123], a[108:111], a[40:43], v[120:123]\n"
    "ds_read2_b32 a[70:71], v20 offset0:50 offset1:51\n"
    "ds_read_b128 a[8:11], v19 offset:1216\n"
    "v_mfma_f32_16x16x32_bf16 v[124:127], a[108:111], a[44:47], v[124:127]\n"
    "ds_read2_b32 a[72:73], v21 offset0:176 offset1:177\n"
    "v_mfma_f32_16x16x32_bf16 v[116:119], a[104:107], a[32:35], v[116:119]\n"
    "ds_read2_b32 a[74:75], v21 offset0:178 offset1:179\n"
    "s_waitcnt lgkmcnt(15)\n"
    "v_mfma_f32_16x16x32_bf16 v[120:123], a[100:103], a[48:51], v[120:123]\n"
    "ds_read_b128 a[12:15], v19 offset:1728\n"
    "v_mfma_f32_16x16x32_bf16 v[124:127], a[104:107], a[48:51], v[124:127]\n"
    "ds_read2_b32 a[76:77], v21 offset0:48 offset1:49\n"
    "ds_read2_b32 a[78:79], v21 offset0:50 offset1:51\n"
    "v_mfma_f32_16x16x32_bf16 v[120:123], a[112:115], a[36:39], v[120:123]\n"
    "ds_read_b128 a[16:19], v19 offset:2240\n"
    "v_mfma_f32_16x16x32_bf16 v[124:127], a[112:115], a[40:43], v[124:127]\n"
    "ds_read2_b32 a[80:81], v45 offset0:176 offset1:177\n"
    "s_waitcnt lgkmcnt(15)\n"
    "v_mfma_f32_16x16x32_bf16 v[120:123], a[96:99], a[52:55], v[120:123]\n"
    "ds_read2_b32 a[82:83], v45 offset0:178 offset1:179\n"
    "v_mfma_f32_16x16x32_bf16 v[120:123], a[116:119], a[32:35], v[120:123]\n"
    "ds_read_b128 a[20:23], v19 offset:2752\n"
    "ds_read2_b32 a[84:85], v45 offset0:48 offset1:49\n"
    "v_mfma_f32_16x16x32_bf16 v[124:127], a[100:103], a[52:55], v[124:127]\n"
    "ds_read2_b32 a[86:87], v45 offset0:50 offset1:51\n"
    "v_mfma_f32_16x16x32_bf16 v[124:127], a[116:119], a[36:39], v[124:127]\n"
    "ds_read_b128 a[24:27], v19 offset:3264\n"
    "s_waitcnt lgkmcnt(15)\n"
    "v_mfma_f32_16x16x32_bf16 v[124:127], a[96:99], a[56:59], v[124:127]\n"
    "ds_read2_b32 a[88:89], v46 offset0:176 offset1:177\n"
    "v_mfma_f32_16x16x32_bf16 v[124:127], a[120:123], a[32:35], v[124:127]\n"
    "ds_read2_b32 a[90:91], v46 offset0:178 offset1:179\n"
    "s_waitcnt lgkmcnt(15)\n"
    "v_mfma_f32_16x16x32_bf16 v[112:115], a[64:67], a[4:7], v[112:115]\n"
    "v_mfma_f32_16x16x32_bf16 v[116:119], a[68:71], a[4:7], v[116:119]\n"
    "ds_read_b128 a[32:35], v19 offset:128\n"
    "ds_read2_b32 a[96:97], v20 offset0:160 offset1:161\n"
    "s_waitcnt lgkmcnt(11)\n"
    "v_mfma_f32_16x16x32_bf16 v[120:123], a[72:75], a[12:15], v[120:123]\n"
    "ds_read2_b32 a[98:99], v20 offset0:162 offset1:163\n"
    "v_mfma_f32_16x16x32_bf16 v[112:115], a[68:71], a[0:3], v[112:115]\n"
    "ds_read_b128 a[36:39], v19 offset:640\n"
    "v_mfma_f32_16x16x32_bf16 v[116:119], a[64:67], a[8:11], v[116:119]\n"
    "ds_read2_b32 a[100:101], v20 offset0:32 offset1:33\n"
    "v_mfma_f32_16x16x32_bf16 v[120:123], a[76:79], a[8:11], v[120:123]\n"
    "ds_read2_b32 a[102:103], v20 offset0:34 offset1:35\n"
    "ds_read_b128 a[40:43], v19 offset:1152\n"
    "v_mfma_f32_16x16x32_bf16 v[124:127], a[76:79], a[12:15], v[124:127]\n"
    "ds_read2_b32 a[104:105], v21 offset0:160 offset1:161\n"
    "v_mfma_f32_16x16x32_bf16 v[116:119], a[72:75], a[0:3], v[116:119]\n"
    "ds_read2_b32 a[106:107], v21 offset0:162 offset1:163\n"
    "s_waitcnt lgkmcnt(15)\n"
    "v_mfma_f32_16x16x32_bf16 v[120:123], a[68:71], a[16:19], v[120:123]\n"
    "ds_read_b128 a[44:47], v19 offset:1664\n"
    "v_mfma_f32_16x16x32_bf16 v[124:127], a[72:75], a[16:19], v[124:127]\n"
    "ds_read2_b32 a[108:109], v21 offset0:32 offset1:33\n"
    "ds_read2_b32 a[110:111], v21 offset0:34 offset1:35\n"
    "v_mfma_f32_16x16x32_bf16 v[120:123], a[80:83], a[4:7], v[120:123]\n"
    "ds_read_b128 a[48:51], v19 offset:2176\n"
    "v_mfma_f32_16x16x32_bf16 v[124:127], a[80:83], a[8:11], v[124:127]\n"
    "ds_read2_b32 a[112:113], v45 offset0:160 offset1:161\n"
    "s_waitcnt lgkmcnt(15)\n"
    "v_mfma_f32_16x16x32_bf16 v[120:123], a[64:67], a[20:23], v[120:123]\n"
    "ds_read2_b32 a[114:115], v45 offset0:162 offset1:163\n"
    "v_mfma_f32_16x16x32_bf16 v[120:123], a[84:87], a[0:3], v[120:123]\n"
    "ds_read_b128 a[52:55], v19 offset:2688\n"
    "ds_read2_b32 a[116:117], v45 offset0:32 offset1:33\n"
    "v_mfma_f32_16x16x32_bf16 v[124:127], a[68:71], a[20:23], v[124:127]\n"
    "ds_read2_b32 a[118:119], v45 offset0:34 offset1:35\n"
    "v_mfma_f32_16x16x32_bf16 v[124:127], a[84:87], a[4:7], v[124:127]\n"
    "ds_read_b128 a[56:59], v19 offset:3200\n"
    "s_waitcnt lgkmcnt(15)\n"
    "v_mfma_f32_16x16x32_bf16 v[124:127], a[64:67], a[24:27], v[124:127]\n"
    "ds_read2_b32 a[120:121], v46 offset0:160 offset1:161\n"
    "v_mfma_f32_16x16x32_bf16 v[124:127], a[88:91], a[0:3], v[124:127]\n"
    "ds_read2_b32 a[122:123], v46 offset0:162 offset1:163\n"
    "s_waitcnt lgkmcnt(15)\n"
    "v_mfma_f32_16x16x32_bf16 v[112:115], a[96:99], a[36:39], v[112:115]\n"
    "v_mfma_f32_16x16x32_bf16 v[116:119], a[100:103], a[36:39], v[116:119]\n"
    "ds_read_b128 a[0:3], v19 offset:64\n"
    "ds_read2_b32 a[64:65], v20 offset0:144 offset1:145\n"
    "s_waitcnt lgkmcnt(11)\n"
    "v_mfma_f32_16x16x32_bf16 v[120:123], a[104:107], a[44:47], v[120:123]\n"
    "ds_read2_b32 a[66:67], v20 offset0:146 offset1:147\n"
    "v_mfma_f32_16x16x32_bf16 v[112:115], a[100:103], a[32:35], v[112:115]\n"
    "ds_read_b128 a[4:7], v19 offset:576\n"
    "v_mfma_f32_16x16x32_bf16 v[116:119], a[96:99], a[40:43], v[116:119]\n"
    "ds_read2_b32 a[68:69], v20 offset0:16 offset1:17\n"
    "v_mfma_f32_16x16x32_bf16 v[120:123], a[108:111], a[40:43], v[120:123]\n"
    "ds_read2_b32 a[70:71], v20 offset0:18 offset1:19\n"
    "ds_read_b128 a[8:11], v19 offset:1088\n"
    "v_mfma_f32_16x16x32_bf16 v[124:127], a[108:111], a[44:47], v[124:127]\n"
    "ds_read2_b32 a[72:73], v21 offset0:144 offset1:145\n"
    "v_mfma_f32_16x16x32_bf16 v[116:119], a[104:107], a[32:35], v[116:119]\n"
    "ds_read2_b32 a[74:75], v21 offset0:146 offset1:147\n"
    "s_waitcnt lgkmcnt(15)\n"
    "v_mfma_f32_16x16x32_bf16 v[120:123], a[100:103], a[48:51], v[120:123]\n"
    "ds_read_b128 a[12:15], v19 offset:1600\n"
    "v_mfma_f32_16x16x32_bf16 v[124:127], a[104:107], a[48:51], v[124:127]\n"
    "ds_read2_b32 a[76:77], v21 offset0:16 offset1:17\n"
    "ds_read2_b32 a[78:79], v21 offset0:18 offset1:19\n"
    "v_mfma_f32_16x16x32_bf16 v[120:123], a[112:115], a[36:39], v[120:123]\n"
    "ds_read_b128 a[16:19], v19 offset:2112\n"
    "v_mfma_f32_16x16x32_bf16 v[124:127], a[112:115], a[40:43], v[124:127]\n"
    "ds_read2_b32 a[80:81], v45 offset0:144 offset1:145\n"
    "s_waitcnt lgkmcnt(15)\n"
    "v_mfma_f32_16x16x32_bf16 v[120:123], a[96:99], a[52:55], v[120:123]\n"
    "ds_read2_b32 a[82:83], v45 offset0:146 offset1:147\n"
    "v_mfma_f32_16x16x32_bf16 v[120:123], a[116:119], a[32:35], v[120:123]\n"
    "ds_read_b128 a[20:23], v19 offset:2624\n"
    "ds_read2_b32 a[84:85], v45 offset0:16 offset1:17\n"
    "v_mfma_f32_16x16x32_bf16 v[124:127], a[100:103], a[52:55], v[124:127]\n"
    "ds_read2_b32 a[86:87], v45 offset0:18 offset1:19\n"
    "v_mfma_f32_16x16x32_bf16 v[124:127], a[116:119], a[36:39], v[124:127]\n"
    "ds_read_b128 a[24:27], v19 offset:3136\n"
    "s_waitcnt lgkmcnt(15)\n"
    "v_mfma_f32_16x16x32_bf16 v[124:127], a[96:99], a[56:59], v[124:127]\n"
    "ds_read2_b32 a[88:89], v46 offset0:144 offset1:145\n"
    "v_mfma_f32_16x16x32_bf16 v[124:127], a[120:123], a[32:35], v[124:127]\n"
    "ds_read2_b32 a[90:91], v46 offset0:146 offset1:147\n"
    "s_waitcnt lgkmcnt(15)\n"
    "v_mfma_f32_16x16x32_bf16 v[112:115], a[64:67], a[4:7], v[112:115]\n"
    "v_mfma_f32_16x16x32_bf16 v[116:119], a[68:71], a[4:7], v[116:119]\n"
    "ds_read_b128 a[32:35], v19 offset:0\n"
    "ds_read2_b32 a[96:97], v20 offset0:128 offset1:129\n"
    "s_waitcnt lgkmcnt(11)\n"
    "v_mfma_f32_16x16x32_bf16 v[120:123], a[72:75], a[12:15], v[120:123]\n"
    "ds_read2_b32 a[98:99], v20 offset0:130 offset1:131\n"
    "v_mfma_f32_16x16x32_bf16 v[112:115], a[68:71], a[0:3], v[112:115]\n"
    "ds_read_b128 a[36:39], v19 offset:512\n"
    "v_mfma_f32_16x16x32_bf16 v[116:119], a[64:67], a[8:11], v[116:119]\n"
    "ds_read2_b32 a[100:101], v20 offset0:0 offset1:1\n"
    "v_mfma_f32_16x16x32_bf16 v[120:123], a[76:79], a[8:11], v[120:123]\n"
    "ds_read2_b32 a[102:103], v20 offset0:2 offset1:3\n"
    "ds_read_b128 a[40:43], v19 offset:1024\n"
    "v_mfma_f32_16x16x32_bf16 v[124:127], a[76:79], a[12:15], v[124:127]\n"
    "ds_read2_b32 a[104:105], v21 offset0:128 offset1:129\n"
    "v_mfma_f32_16x16x32_bf16 v[116:119], a[72:75], a[0:3], v[116:119]\n"
    "ds_read2_b32 a[106:107], v21 offset0:130 offset1:131\n"
    "s_waitcnt lgkmcnt(15)\n"
    "v_mfma_f32_16x16x32_bf16 v[120:123], a[68:71], a[16:19], v[120:123]\n"
    "ds_read_b128 a[44:47], v19 offset:1536\n"
    "v_mfma_f32_16x16x32_bf16 v[124:127], a[72:75], a[16:19], v[124:127]\n"
    "ds_read2_b32 a[108:109], v21 offset0:0 offset1:1\n"
    "ds_read2_b32 a[110:111], v21 offset0:2 offset1:3\n"
    "v_mfma_f32_16x16x32_bf16 v[120:123], a[80:83], a[4:7], v[120:123]\n"
    "ds_read_b128 a[48:51], v19 offset:2048\n"
    "v_mfma_f32_16x16x32_bf16 v[124:127], a[80:83], a[8:11], v[124:127]\n"
    "ds_read2_b32 a[112:113], v45 offset0:128 offset1:129\n"
    "s_waitcnt lgkmcnt(15)\n"
    "v_mfma_f32_16x16x32_bf16 v[120:123], a[64:67], a[20:23], v[120:123]\n"
    "ds_read2_b32 a[114:115], v45 offset0:130 offset1:131\n"
    "v_mfma_f32_16x16x32_bf16 v[120:123], a[84:87], a[0:3], v[120:123]\n"
    "ds_read_b128 a[52:55], v19 offset:2560\n"
    "ds_read2_b32 a[116:117], v45 offset0:0 offset1:1\n"
    "v_mfma_f32_16x16x32_bf16 v[124:127], a[68:71], a[20:23], v[124:127]\n"
    "ds_read2_b32 a[118:119], v45 offset0:2 offset1:3\n"
    "v_mfma_f32_16x16x32_bf16 v[124:127], a[84:87], a[4:7], v[124:127]\n"
    "ds_read_b128 a[56:59], v19 offset:3072\n"
    "s_waitcnt lgkmcnt(15)\n"
    "v_mfma_f32_16x16x32_bf16 v[124:127], a[64:67], a[24:27], v[124:127]\n"
    "ds_read2_b32 a[120:121], v46 offset0:128 offset1:129\n"
    "v_mfma_f32_16x16x32_bf16 v[124:127], a[88:91], a[0:3], v[124:127]\n"
    "ds_read2_b32 a[122:123], v46 offset0:130 offset1:131\n"
    "s_waitcnt lgkmcnt(15)\n"
    "v_mfma_f32_16x16x32_bf16 v[112:115], a[96:99], a[36:39], v[112:115]\n"
    "v_mfma_f32_16x16x32_bf16 v[116:119], a[100:103], a[36:39], v[116:119]\n"
    "s_waitcnt lgkmcnt(9)\n"
    "v_mfma_f32_16x16x32_bf16 v[120:123], a[104:107], a[44:47], v[120:123]\n"
    "v_mfma_f32_16x16x32_bf16 v[112:115], a[100:103], a[32:35], v[112:115]\n"
    "v_mfma_f32_16x16x32_bf16 v[116:119], a[96:99], a[40:43], v[116:119]\n"
    "v_mfma_f32_16x16x32_bf16 v[120:123], a[108:111], a[40:43], v[120:123]\n"
    "v_mfma_f32_16x16x32_bf16 v[124:127], a[108:111], a[44:47], v[124:127]\n"
    "v_mfma_f32_16x16x32_bf16 v[116:119], a[104:107], a[32:35], v[116:119]\n"
    "s_waitcnt lgkmcnt(6)\n"
    "v_mfma_f32_16x16x32_bf16 v[120:123], a[100:103], a[48:51], v[120:123]\n"
    "v_mfma_f32_16x16x32_bf16 v[124:127], a[104:107], a[48:51], v[124:127]\n"
    "v_mfma_f32_16x16x32_bf16 v[120:123], a[112:115], a[36:39], v[120:123]\n"
    "v_mfma_f32_16x16x32_bf16 v[124:127], a[112:115], a[40:43], v[124:127]\n"
    "s_waitcnt lgkmcnt(3)\n"
    "v_mfma_f32_16x16x32_bf16 v[120:123], a[96:99], a[52:55], v[120:123]\n"
    "v_mfma_f32_16x16x32_bf16 v[120:123], a[116:119], a[32:35], v[120:123]\n"
    "v_mfma_f32_16x16x32_bf16 v[124:127], a[100:103], a[52:55], v[124:127]\n"
    "v_mfma_f32_16x16x32_bf16 v[124:127], a[116:119], a[36:39], v[124:127]\n"
    "s_waitcnt lgkmcnt(0)\n"
    "v_mfma_f32_16x16x32_bf16 v[124:127], a[96:99], a[56:59], v[124:127]\n"
    "v_mfma_f32_16x16x32_bf16 v[124:127], a[120:123], a[32:35], v[124:127]\n"
    "s_nop 7\n"
    "s_branch .Leven_%=\n"
    ".LG_%=:\n"
    "s_and_b32 s14, s2, 7\n"
    "s_lshr_b32 s15, s2, 3\n"
    "s_lshr_b32 s8, s15, 2\n"
    "s_and_b32 s9, s15, 3\n"
    "s_lshl_b32 s7, s14, 2\n"
    "s_or_b32 s7, s7, s9\n"
    "s_lshr_b32 s4, s14, 1\n"
    "s_lshl_b32 s4, s4, 2\n"
    "s_or_b32 s4, s4, s9\n"
    "s_and_b32 s5, s14, 1\n"
    "s_lshl_b32 s5, s5, 3\n"
    "s_or_b32 s5, s5, s8\n"
    "s_mul_i32 s6, s5, 43\n"
    "s_add_u32 s9, s6, 43\n"
    "s_lshr_b32 s6, s6, 4\n"
    "s_lshr_b32 s9, s9, 4\n"
    "s_sub_u32 s9, s9, s6\n"
    "s_cmp_eq_u32 s9, 2\n"
    "s_cselect_b64 s[82:83], -1, 0\n"
    "s_lshr_b32 s9, s5, 1\n"
    "s_lshl_b32 s61, s6, 6\n"
    "v_lshrrev_b32 v5, 4, v1\n"
    "v_lshlrev_b32 v6, 4, v1\n"
    "v_mov_b32 v7, 0x80000000\n"
    "v_and_b32 v2, 31, v1\n"
    "v_lshrrev_b32 v3, 5, v1\n"
    "v_lshl_add_u32 v8, v4, 2, v5\n"
    "v_xor_b32 v8, v8, v1\n"
    "v_and_b32 v8, 7, v8\n"
    "v_lshlrev_b32 v8, 4, v8\n"
    "v_lshl_add_u32 v10, v4, 3, v9\n"
    "v_lshl_or_b32 v8, v10, 10, v8\n"
    "v_lshl_add_u32 v95, s4, 17, v8\n"
    "s_lshl_b32 s39, s3, 10\n"
    "s_mov_b32 s40, 43691\n"
    "s_movk_i32 s41, 2752\n"
    "s_not_b64 s[10:11], s[82:83]\n"
    "s_add_u32 s12, s5, 0\n"
    "s_and_b32 s12, s12, 3\n"
    "v_lshl_add_u32 v88, s12, 15, v95\n"
    "s_lshl_b32 s12, s12, 12\n"
    "s_add_u32 s50, s12, s39\n"
    "s_and_b32 s12, s4, 3\n"
    "s_add_u32 s12, s12, 0\n"
    "s_mul_i32 s13, s12, 11\n"
    "s_lshr_b32 s13, s13, 5\n"
    "s_mul_i32 s13, s13, 3\n"
    "s_sub_u32 s12, s12, s13\n"
    "s_lshl_b32 s12, s12, 12\n"
    "s_add_u32 s54, s12, s39\n"
    "s_lshr_b32 s12, s54, 4\n"
    "v_add_u32 v10, s12, v1\n"
    "v_mul_u32_u24 v8, s40, v10\n"
    "v_lshrrev_b32 v8, 19, v8\n"
    "v_mul_u32_u24 v11, 12, v8\n"
    "v_sub_u32 v11, v10, v11\n"
    "v_cmp_gt_u32 s[84:85], 8, v11\n"
    "v_lshl_add_u32 v11, v11, 4, s61\n"
    "v_mad_u32_u24 v92, v8, s41, v11\n"
    "s_or_b64 s[84:85], s[84:85], s[10:11]\n"
    "s_waitcnt lgkmcnt(0)\n"
    "s_lshl_b32 s57, s9, 7\n"
    "s_mul_i32 s92, s57, 1376\n"
    "s_add_u32 m0, s50, 48000\n"
    "s_nop 0\n"
    "buffer_load_dwordx4 v88, s[16:19], s57 offen lds\n"
    "s_add_u32 m0, s54, 113536\n"
    "s_mov_b64 exec, s[84:85]\n"
    "buffer_load_dwordx4 v92, s[20:23], s92 offen lds\n"
    "s_mov_b64 exec, -1\n"
    "s_add_u32 s58, s57, 128\n"
    "s_and_b32 s58, s58, 1023\n"
    "s_mul_i32 s93, s58, 1376\n"
    "s_add_u32 m0, s50, 64384\n"
    "s_nop 0\n"
    "buffer_load_dwordx4 v88, s[16:19], s58 offen lds\n"
    "s_add_u32 m0, s54, 125824\n"
    "s_mov_b64 exec, s[84:85]\n"
    "buffer_load_dwordx4 v92, s[20:23], s93 offen lds\n"
    "s_mov_b64 exec, -1\n"
    "s_load_dword s80, s[24:25], 0x0\n"
    "s_add_u32 s12, s5, 1\n"
    "s_and_b32 s12, s12, 3\n"
    "v_lshl_add_u32 v89, s12, 15, v95\n"
    "s_lshl_b32 s12, s12, 12\n"
    "s_add_u32 s51, s12, s39\n"
    "s_add_u32 s12, s5, 2\n"
    "s_and_b32 s12, s12, 3\n"
    "v_lshl_add_u32 v90, s12, 15, v95\n"
    "s_lshl_b32 s12, s12, 12\n"
    "s_add_u32 s52, s12, s39\n"
    "s_add_u32 s12, s5, 3\n"
    "s_and_b32 s12, s12, 3\n"
    "v_lshl_add_u32 v91, s12, 15, v95\n"
    "s_lshl_b32 s12, s12, 12\n"
    "s_add_u32 s53, s12, s39\n"
    "s_and_b32 s12, s4, 3\n"
    "s_add_u32 s12, s12, 1\n"
    "s_mul_i32 s13, s12, 11\n"
    "s_lshr_b32 s13, s13, 5\n"
    "s_mul_i32 s13, s13, 3\n"
    "s_sub_u32 s12, s12, s13\n"
    "s_lshl_b32 s12, s12, 12\n"
    "s_add_u32 s55, s12, s39\n"
    "s_lshr_b32 s12, s55, 4\n"
    "v_add_u32 v10, s12, v1\n"
    "v_mul_u32_u24 v8, s40, v10\n"
    "v_lshrrev_b32 v8, 19, v8\n"
    "v_mul_u32_u24 v11, 12, v8\n"
    "v_sub_u32 v11, v10, v11\n"
    "v_cmp_gt_u32 s[86:87], 8, v11\n"
    "v_lshl_add_u32 v11, v11, 4, s61\n"
    "v_mad_u32_u24 v93, v8, s41, v11\n"
    "s_or_b64 s[86:87], s[86:87], s[10:11]\n"
    "s_and_b32 s12, s4, 3\n"
    "s_add_u32 s12, s12, 2\n"
    "s_mul_i32 s13, s12, 11\n"
    "s_lshr_b32 s13, s13, 5\n"
    "s_mul_i32 s13, s13, 3\n"
    "s_sub_u32 s12, s12, s13\n"
    "s_lshl_b32 s12, s12, 12\n"
    "s_add_u32 s56, s12, s39\n"
    "s_lshr_b32 s12, s56, 4\n"
    "v_add_u32 v10, s12, v1\n"
    "v_mul_u32_u24 v8, s40, v10\n"
    "v_lshrrev_b32 v8, 19, v8\n"
    "v_mul_u32_u24 v11, 12, v8\n"
    "v_sub_u32 v11, v10, v11\n"
    "v_cmp_gt_u32 s[88:89], 8, v11\n"
    "v_lshl_add_u32 v11, v11, 4, s61\n"
    "v_mad_u32_u24 v94, v8, s41, v11\n"
    "s_or_b64 s[88:89], s[88:89], s[10:11]\n"
    "s_mul_i32 s47, s3, 2048\n"
    "s_add_u32 s47, s47, 39808\n"
    "s_lshl_b32 s12, s3, 5\n"
    "s_add_u32 s12, s12, s15\n"
    "s_cmp_lt_u32 s12, 86\n"
    "s_cselect_b64 vcc, -1, 0\n"
    "v_and_b32 v11, 112, v6\n"
    "v_lshl_or_b32 v8, v9, 10, v11\n"
    "v_cndmask_b32 v19, v7, v8, vcc\n"
    "v_add_u32 v20, 7168, v19\n"
    "v_cndmask_b32 v21, v7, v6, vcc\n"
    "v_lshlrev_b32 v22, 1, v1\n"
    "v_lshlrev_b32 v23, 1, v2\n"
    "v_cndmask_b32 v23, v23, v7, s[82:83]\n"
    "v_lshlrev_b32 v24, 2, v0\n"
    "v_cndmask_b32 v24, v7, v24, s[82:83]\n"
    "s_lshl_b32 s13, s12, 14\n"
    "s_lshl_b32 s10, s14, 7\n"
    "s_add_u32 s13, s13, s10\n"
    "s_mov_b32 m0, s47\n"
    "s_nop 0\n"
    "buffer_load_dwordx4 v19, s[28:31], s13 offen lds\n"
    "buffer_load_dwordx4 v20, s[28:31], s13 offen offset:1024 lds\n"
    "buffer_load_dword v96, v24, s[68:71], 0 offen\n"
    "buffer_load_ushort v120, v22, s[64:67], s61 offen\n"
    "buffer_load_ushort v124, v23, s[64:67], s61 offen offset:128\n"
    "s_mul_i32 s41, s14, 172\n"
    "s_add_u32 s41, s41, s12\n"
    "s_lshl_b32 s41, s41, 10\n"
    "s_add_u32 s41, s41, 4358144\n"
    "s_add_u32 s42, s41, 88064\n"
    "v_bfe_u32 v13, v1, 2, 2\n"
    "v_and_b32 v16, 3, v1\n"
    "v_bfe_u32 v17, v1, 4, 1\n"
    "v_lshlrev_b32 v18, 3, v16\n"
    "v_lshl_or_b32 v18, v17, 5, v18\n"
    "v_bfe_u32 v9, v1, 1, 3\n"
    "v_xor_b32 v9, v9, v3\n"
    "v_lshlrev_b32 v9, 4, v9\n"
    "v_lshl_add_u32 v10, v4, 5, v2\n"
    "v_lshl_or_b32 v9, v10, 7, v9\n"
    "v_add_u32 v19, 48000, v9\n"
    "v_xor_b32 v20, 32, v9\n"
    "v_add_u32 v20, 48000, v20\n"
    "v_xor_b32 v22, 64, v9\n"
    "v_add_u32 v22, 48000, v22\n"
    "v_xor_b32 v23, 96, v9\n"
    "v_add_u32 v23, 48000, v23\n"
    "v_lshl_add_u32 v9, v3, 3, v13\n"
    "v_mul_u32_u24 v9, 192, v9\n"
    "v_add_u32 v9, 113536, v9\n"
    "v_add_u32 v24, v9, v18\n"
    "v_lshl_add_u32 v9, v3, 2, v13\n"
    "v_lshlrev_b32 v10, 3, v9\n"
    "v_add_u32 v10, 113536, v10\n"
    "v_cndmask_b32 v25, v24, v10, s[82:83]\n"
    "v_add_u32 v10, 192, v10\n"
    "v_cndmask_b32 v26, v24, v10, s[82:83]\n"
    "v_lshl_add_u32 v14, v9, 7, v18\n"
    "v_add_u32 v14, s47, v14\n"
    "v_lshl_add_u32 v8, v4, 1, v3\n"
    "v_subrev_u32 v8, s9, v8\n"
    "s_add_u32 m0, s51, 48000\n"
    "s_nop 0\n"
    "buffer_load_dwordx4 v89, s[16:19], s57 offen lds\n"
    "v_and_b32 v9, 3, v8\n"
    "v_mul_u32_u24 v9, 12288, v9\n"
    "v_bfe_u32 v8, v8, 2, 1\n"
    "s_add_u32 m0, s55, 113536\n"
    "s_mov_b64 exec, s[86:87]\n"
    "buffer_load_dwordx4 v93, s[20:23], s92 offen lds\n"
    "s_mov_b64 exec, -1\n"
    "v_mul_u32_u24 v8, 192, v8\n"
    "v_bfe_u32 v10, v1, 3, 2\n"
    "v_bfe_u32 v11, v1, 1, 1\n"
    "s_add_u32 m0, s52, 48000\n"
    "s_nop 0\n"
    "buffer_load_dwordx4 v90, s[16:19], s57 offen lds\n"
    "v_lshl_add_u32 v10, v10, 2, v11\n"
    "v_mul_u32_u24 v10, 768, v10\n"
    "v_add3_u32 v8, v8, v9, v10\n"
    "s_add_u32 m0, s56, 113536\n"
    "s_mov_b64 exec, s[88:89]\n"
    "buffer_load_dwordx4 v94, s[20:23], s92 offen lds\n"
    "s_mov_b64 exec, -1\n"
    "v_bfe_u32 v10, v1, 2, 1\n"
    "v_and_b32 v11, 1, v1\n"
    "v_lshl_add_u32 v10, v10, 1, v11\n"
    "s_add_u32 m0, s53, 48000\n"
    "s_nop 0\n"
    "buffer_load_dwordx4 v91, s[16:19], s57 offen lds\n"
    "v_lshl_add_u32 v8, v10, 4, v8\n"
    "v_add_u32 v15, 113664, v8\n"
    "v_mov_b32 v121, 0\n"
    "s_add_u32 m0, s51, 64384\n"
    "s_nop 0\n"
    "buffer_load_dwordx4 v89, s[16:19], s58 offen lds\n"
    "v_mov_b32 v122, 0\n"
    "v_mov_b32 v123, 0\n"
    "v_mov_b32 v125, 0\n"
    "s_add_u32 m0, s55, 125824\n"
    "s_mov_b64 exec, s[86:87]\n"
    "buffer_load_dwordx4 v93, s[20:23], s93 offen lds\n"
    "s_mov_b64 exec, -1\n"
    "v_mov_b32 v126, 0\n"
    "v_mov_b32 v127, 0\n"
    "v_mov_b32 v113, 0\n"
    "s_add_u32 m0, s52, 64384\n"
    "s_nop 0\n"
    "buffer_load_dwordx4 v90, s[16:19], s58 offen lds\n"
    "v_mov_b32 v114, 0\n"
    "v_mov_b32 v115, 0\n"
    "v_mov_b32 v117, 0\n"
    "s_add_u32 m0, s56, 125824\n"
    "s_mov_b64 exec, s[88:89]\n"
    "buffer_load_dwordx4 v94, s[20:23], s93 offen lds\n"
    "s_mov_b64 exec, -1\n"
    "v_mov_b32 v118, 0\n"
    "v_mov_b32 v119, 0\n"
    "v_sub_u32 v112, 1, v3\n"
    "s_add_u32 m0, s53, 64384\n"
    "s_nop 0\n"
    "buffer_load_dwordx4 v91, s[16:19], s58 offen lds\n"
    "v_mul_u32_u24 v112, 0x3f80, v112\n"
    "v_mul_u32_u24 v116, 0x3f80, v3\n"
    "s_and_b32 s12, s2, 7\n"
    "s_lshl_b32 s12, s12, 5\n"
    "s_lshr_b32 s13, s2, 3\n"
    "s_add_u32 s12, s12, s13\n"
    "s_lshl_b32 s12, s12, 3\n"
    "s_add_u32 s12, s12, 1024\n"
    "s_mov_b32 s62, s12\n"
    "s_movk_i32 s33, 4096\n"
    "s_movk_i32 s34, 8192\n"
    "s_movk_i32 s35, 12288\n"
    "s_movk_i32 s36, 16384\n"
    "s_movk_i32 s37, 20480\n"
    "s_mul_i32 s44, s3, 22\n"
    "s_cmp_eq_u32 s3, 3\n"
    "s_cselect_b32 s44, 65, s44\n"
    "s_cmp_ge_u32 s3, 2\n"
    "s_cselect_b64 s[48:49], -1, 0\n"
    "s_and_b32 s13, s3, 1\n"
    "s_xor_b32 s13, s13, 0\n"
    "s_lshl_b32 s14, s8, 1\n"
    "s_add_u32 s13, s13, s14\n"
    "s_mul_i32 s13, s13, 86\n"
    "s_add_u32 s13, s13, s44\n"
    "s_lshl_b32 s13, s13, 10\n"
    "s_add_u32 s45, s13, 4358144\n"
    "s_and_b32 s13, s3, 1\n"
    "s_xor_b32 s13, s13, 1\n"
    "s_lshl_b32 s14, s8, 1\n"
    "s_add_u32 s13, s13, s14\n"
    "s_mul_i32 s13, s13, 86\n"
    "s_add_u32 s13, s13, s44\n"
    "s_lshl_b32 s13, s13, 10\n"
    "s_add_u32 s46, s13, 4358144\n"
    "s_lshl_b32 s12, s4, 2\n"
    "s_add_u32 s12, s12, s3\n"
    "s_mul_i32 s13, s12, 86\n"
    "s_lshl_b32 s14, s6, 1\n"
    "s_add_u32 s13, s13, s14\n"
    "s_lshl_b32 s13, s13, 10\n"
    "s_add_u32 s90, s13, 5767168\n"
    "s_lshl_b32 s12, s12, 7\n"
    "s_add_u32 s12, s12, 16384\n"
    "s_cmp_eq_u32 s5, 0\n"
    "s_cselect_b32 s91, s12, 0x80000000\n"
    "s_lshl_b32 s12, s4, 4\n"
    "s_add_u32 s12, s12, s5\n"
    "s_lshl_b32 s12, s12, 2\n"
    "s_add_u32 s12, s12, s3\n"
    "s_lshl_b32 s12, s12, 7\n"
    "s_add_u32 s43, s12, 32768\n"
    "s_mov_b32 s12, 0x01000100\n"
    "s_mov_b32 s13, 0x03020302\n"
    "s_waitcnt vmcnt(13)\n"
    "s_waitcnt lgkmcnt(0)\n"
    "s_add_u32 s80, s80, 1\n"
    "v_mov_b32 v12, s80\n"
    "ds_read_b64_tr_b16 v[104:105], v14 offset:0\n"
    "ds_read_b64_tr_b16 v[106:107], v14 offset:1024\n"
    "ds_read_b64_tr_b16 v[108:109], v14 offset:64\n"
    "ds_read_b64_tr_b16 v[110:111], v14 offset:1088\n"
    "s_waitcnt vmcnt(10)\n"
    "v_perm_b32 v98, v96, v96, s13\n"
    "v_perm_b32 v96, v96, v96, s12\n"
    "v_mov_b32 v99, v98\n"
    "v_mov_b32 v97, v96\n"
    "s_mov_b64 exec, s[82:83]\n"
    "ds_write_b128 v15, v[96:99]\n"
    "s_mov_b64 exec, -1\n"
    "v_mfma_f32_32x32x16_bf16 v[40:55], v[120:123], v[112:115], 0\n"
    "v_mfma_f32_32x32x16_bf16 v[56:71], v[120:123], v[116:119], 0\n"
    "v_mfma_f32_32x32x16_bf16 v[72:87], v[124:127], v[112:115], 0\n"
    "s_waitcnt lgkmcnt(0)\n"
    "buffer_store_dwordx4 v[104:107], v21, s[24:27], s41 offen sc1\n"
    "buffer_store_dwordx4 v[108:111], v21, s[24:27], s42 offen sc1\n"
    "s_add_u32 s59, s58, 128\n"
    "s_and_b32 s59, s59, 1023\n"
    "s_mul_i32 s94, s59, 1376\n"
    "s_add_u32 m0, s50, 80768\n"
    "s_nop 0\n"
    "buffer_load_dwordx4 v88, s[16:19], s59 offen lds\n"
    "s_add_u32 m0, s54, 138112\n"
    "s_mov_b64 exec, s[84:85]\n"
    "buffer_load_dwordx4 v92, s[20:23], s94 offen lds\n"
    "s_mov_b64 exec, -1\n"
    "s_add_u32 s60, s59, 128\n"
    "s_and_b32 s60, s60, 1023\n"
    "s_mul_i32 s95, s60, 1376\n"
    "s_add_u32 m0, s50, 97152\n"
    "s_nop 0\n"
    "buffer_load_dwordx4 v88, s[16:19], s60 offen lds\n"
    "s_add_u32 m0, s54, 150400\n"
    "s_mov_b64 exec, s[84:85]\n"
    "buffer_load_dwordx4 v92, s[20:23], s95 offen lds\n"
    "s_mov_b64 exec, -1\n"
    "s_waitcnt vmcnt(11)\n"
    "s_barrier\n"
    "ds_read_b128 a[0:3], v19 offset:0\n"
    "ds_read_b64_tr_b16 a[4:5], v24 offset:0\n"
    "ds_read_b64_tr_b16 a[6:7], v24 offset:768\n"
    "ds_read_b64_tr_b16 a[8:9], v24 offset:64\n"
    "ds_read_b64_tr_b16 a[10:11], v24 offset:832\n"
    "ds_read_b64_tr_b16 a[12:13], v25 offset:128\n"
    "ds_read_b64_tr_b16 a[14:15], v25 offset:896\n"
    "ds_read_b128 a[16:19], v20 offset:0\n"
    "ds_read_b64_tr_b16 a[20:21], v24 offset:3072\n"
    "ds_read_b64_tr_b16 a[22:23], v24 offset:3840\n"
    "ds_read_b64_tr_b16 a[24:25], v24 offset:3136\n"
    "ds_read_b64_tr_b16 a[26:27], v24 offset:3904\n"
    "ds_read_b64_tr_b16 a[28:29], v25 offset:3200\n"
    "ds_read_b64_tr_b16 a[30:31], v25 offset:3968\n"
    "s_waitcnt lgkmcnt(7)\n"
    "v_mfma_f32_32x32x16_bf16 v[40:55], a[4:7], a[0:3], v[40:55]\n"
    "s_add_u32 m0, s51, 80768\n"
    "s_nop 0\n"
    "buffer_load_dwordx4 v89, s[16:19], s59 offen lds\n"
    "ds_read_b128 a[32:35], v22 offset:0\n"
    "ds_read_b64_tr_b16 a[36:37], v24 offset:6144\n"
    "ds_read_b64_tr_b16 a[38:39], v24 offset:6912\n"
    "v_mfma_f32_32x32x16_bf16 v[56:71], a[8:11], a[0:3], v[56:71]\n"
    "s_add_u32 m0, s55, 138112\n"
    "s_mov_b64 exec, s[86:87]\n"
    "buffer_load_dwordx4 v93, s[20:23], s94 offen lds\n"
    "s_mov_b64 exec, -1\n"
    "ds_read_b64_tr_b16 a[40:41], v24 offset:6208\n"
    "ds_read_b64_tr_b16 a[42:43], v24 offset:6976\n"
    "v_mfma_f32_32x32x16_bf16 v[72:87], a[12:15], a[0:3], v[72:87]\n"
    "s_add_u32 m0, s52, 80768\n"
    "s_nop 0\n"
    "buffer_load_dwordx4 v90, s[16:19], s59 offen lds\n"
    "ds_read_b64_tr_b16 a[44:45], v25 offset:6272\n"
    "ds_read_b64_tr_b16 a[46:47], v25 offset:7040\n"
    "s_waitcnt lgkmcnt(7)\n"
    "v_mfma_f32_32x32x16_bf16 v[40:55], a[20:23], a[16:19], v[40:55]\n"
    "ds_read_b128 a[0:3], v23 offset:0\n"
    "ds_read_b64_tr_b16 a[4:5], v24 offset:9216\n"
    "ds_read_b64_tr_b16 a[6:7], v24 offset:9984\n"
    "v_mfma_f32_32x32x16_bf16 v[56:71], a[24:27], a[16:19], v[56:71]\n"
    "s_add_u32 m0, s56, 138112\n"
    "s_mov_b64 exec, s[88:89]\n"
    "buffer_load_dwordx4 v94, s[20:23], s94 offen lds\n"
    "s_mov_b64 exec, -1\n"
    "ds_read_b64_tr_b16 a[8:9], v24 offset:9280\n"
    "ds_read_b64_tr_b16 a[10:11], v24 offset:10048\n"
    "v_mfma_f32_32x32x16_bf16 v[72:87], a[28:31], a[16:19], v[72:87]\n"
    "s_add_u32 m0, s53, 80768\n"
    "s_nop 0\n"
    "buffer_load_dwordx4 v91, s[16:19], s59 offen lds\n"
    "ds_read_b64_tr_b16 a[12:13], v25 offset:9344\n"
    "ds_read_b64_tr_b16 a[14:15], v25 offset:10112\n"
    "s_waitcnt lgkmcnt(7)\n"
    "v_mfma_f32_32x32x16_bf16 v[40:55], a[36:39], a[32:35], v[40:55]\n"
    "s_waitcnt lgkmcnt(0)\n"
    "s_waitcnt vmcnt(11)\n"
    "s_barrier\n"
    "v_mfma_f32_32x32x16_bf16 v[56:71], a[40:43], a[32:35], v[56:71]\n"
    "s_add_u32 s57, s60, 128\n"
    "s_and_b32 s57, s57, 1023\n"
    "s_mul_i32 s92, s57, 1376\n"
    "s_add_u32 m0, s50, 48000\n"
    "s_nop 0\n"
    "buffer_load_dwordx4 v88, s[16:19], s57 offen lds\n"
    "ds_read_b128 a[16:19], v19 offset:16384\n"
    "ds_read_b64_tr_b16 a[20:21], v24 offset:12288\n"
    "ds_read_b64_tr_b16 a[22:23], v24 offset:13056\n"
    "ds_read_b64_tr_b16 a[24:25], v24 offset:12352\n"
    "v_mfma_f32_32x32x16_bf16 v[72:87], a[44:47], a[32:35], v[72:87]\n"
    "s_add_u32 m0, s54, 113536\n"
    "s_mov_b64 exec, s[84:85]\n"
    "buffer_load_dwordx4 v92, s[20:23], s92 offen lds\n"
    "s_mov_b64 exec, -1\n"
    "ds_read_b64_tr_b16 a[26:27], v24 offset:13120\n"
    "ds_read_b64_tr_b16 a[28:29], v25 offset:12416\n"
    "ds_read_b64_tr_b16 a[30:31], v25 offset:13184\n"
    "v_mfma_f32_32x32x16_bf16 v[40:55], a[4:7], a[0:3], v[40:55]\n"
    "ds_read_b128 a[32:35], v20 offset:16384\n"
    "ds_read_b64_tr_b16 a[36:37], v24 offset:15360\n"
    "ds_read_b64_tr_b16 a[38:39], v24 offset:16128\n"
    "v_mfma_f32_32x32x16_bf16 v[56:71], a[8:11], a[0:3], v[56:71]\n"
    "s_add_u32 m0, s51, 97152\n"
    "s_nop 0\n"
    "buffer_load_dwordx4 v89, s[16:19], s60 offen lds\n"
    "ds_read_b64_tr_b16 a[40:41], v24 offset:15424\n"
    "ds_read_b64_tr_b16 a[42:43], v24 offset:16192\n"
    "v_mfma_f32_32x32x16_bf16 v[72:87], a[12:15], a[0:3], v[72:87]\n"
    "s_add_u32 m0, s55, 150400\n"
    "s_mov_b64 exec, s[86:87]\n"
    "buffer_load_dwordx4 v93, s[20:23], s95 offen lds\n"
    "s_mov_b64 exec, -1\n"
    "ds_read_b64_tr_b16 a[44:45], v25 offset:15488\n"
    "ds_read_b64_tr_b16 a[46:47], v25 offset:16256\n"
    "s_waitcnt lgkmcnt(7)\n"
    "v_mfma_f32_32x32x16_bf16 v[40:55], a[20:23], a[16:19], v[40:55]\n"
    "ds_read_b128 a[0:3], v22 offset:16384\n"
    "ds_read_b64_tr_b16 a[4:5], v24 offset:18432\n"
    "ds_read_b64_tr_b16 a[6:7], v24 offset:19200\n"
    "v_mfma_f32_32x32x16_bf16 v[56:71], a[24:27], a[16:19], v[56:71]\n"
    "s_add_u32 m0, s52, 97152\n"
    "s_nop 0\n"
    "buffer_load_dwordx4 v90, s[16:19], s60 offen lds\n"
    "ds_read_b64_tr_b16 a[8:9], v24 offset:18496\n"
    "ds_read_b64_tr_b16 a[10:11], v24 offset:19264\n"
    "v_mfma_f32_32x32x16_bf16 v[72:87], a[28:31], a[16:19], v[72:87]\n"
    "s_add_u32 m0, s56, 150400\n"
    "s_mov_b64 exec, s[88:89]\n"
    "buffer_load_dwordx4 v94, s[20:23], s95 offen lds\n"
    "s_mov_b64 exec, -1\n"
    "ds_read_b64_tr_b16 a[12:13], v25 offset:18560\n"
    "ds_read_b64_tr_b16 a[14:15], v25 offset:19328\n"
    "s_waitcnt lgkmcnt(7)\n"
    "v_mfma_f32_32x32x16_bf16 v[40:55], a[36:39], a[32:35], v[40:55]\n"
    "ds_read_b128 a[16:19], v23 offset:16384\n"
    "ds_read_b64_tr_b16 a[20:21], v24 offset:21504\n"
    "ds_read_b64_tr_b16 a[22:23], v24 offset:22272\n"
    "v_mfma_f32_32x32x16_bf16 v[56:71], a[40:43], a[32:35], v[56:71]\n"
    "s_add_u32 m0, s53, 97152\n"
    "s_nop 0\n"
    "buffer_load_dwordx4 v91, s[16:19], s60 offen lds\n"
    "ds_read_b64_tr_b16 a[24:25], v24 offset:21568\n"
    "ds_read_b64_tr_b16 a[26:27], v24 offset:22336\n"
    "v_mfma_f32_32x32x16_bf16 v[72:87], a[44:47], a[32:35], v[72:87]\n"
    "ds_read_b64_tr_b16 a[28:29], v25 offset:21632\n"
    "ds_read_b64_tr_b16 a[30:31], v25 offset:22400\n"
    "s_waitcnt lgkmcnt(7)\n"
    "v_mfma_f32_32x32x16_bf16 v[40:55], a[4:7], a[0:3], v[40:55]\n"
    "s_waitcnt lgkmcnt(0)\n"
    "s_waitcnt vmcnt(7)\n"
    "s_barrier\n"
    "v_mfma_f32_32x32x16_bf16 v[56:71], a[8:11], a[0:3], v[56:71]\n"
    "s_cmp_lg_u32 s3, 0\n"
    "s_cbranch_scc1 .Lskip2_%=\n"
    "s_mov_b64 exec, 1\n"
    "buffer_store_dword v12, off, s[24:27], s62 sc1\n"
    "s_mov_b64 exec, -1\n"
    ".Lskip2_%=:\n"
    "ds_read_b128 a[32:35], v19 offset:32768\n"
    "ds_read_b64_tr_b16 a[36:37], v24 offset:24576\n"
    "ds_read_b64_tr_b16 a[38:39], v24 offset:25344\n"
    "ds_read_b64_tr_b16 a[40:41], v24 offset:24640\n"
    "v_mfma_f32_32x32x16_bf16 v[72:87], a[12:15], a[0:3], v[72:87]\n"
    "s_add_u32 s58, s57, 128\n"
    "s_and_b32 s58, s58, 1023\n"
    "s_mul_i32 s93, s58, 1376\n"
    "s_add_u32 m0, s50, 64384\n"
    "s_nop 0\n"
    "buffer_load_dwordx4 v88, s[16:19], s58 offen lds\n"
    "ds_read_b64_tr_b16 a[42:43], v24 offset:25408\n"
    "ds_read_b64_tr_b16 a[44:45], v25 offset:24704\n"
    "ds_read_b64_tr_b16 a[46:47], v25 offset:25472\n"
    "v_mfma_f32_32x32x16_bf16 v[40:55], a[20:23], a[16:19], v[40:55]\n"
    "s_add_u32 m0, s54, 125824\n"
    "s_mov_b64 exec, s[84:85]\n"
    "buffer_load_dwordx4 v92, s[20:23], s93 offen lds\n"
    "s_mov_b64 exec, -1\n"
    "ds_read_b128 a[0:3], v20 offset:32768\n"
    "ds_read_b64_tr_b16 a[4:5], v24 offset:27648\n"
    "ds_read_b64_tr_b16 a[6:7], v24 offset:28416\n"
    "v_mfma_f32_32x32x16_bf16 v[56:71], a[24:27], a[16:19], v[56:71]\n"
    "s_lshl_b32 s12, s8, 8\n"
    "ds_read_b64_tr_b16 a[8:9], v24 offset:27712\n"
    "ds_read_b64_tr_b16 a[10:11], v24 offset:28480\n"
    "v_mfma_f32_32x32x16_bf16 v[72:87], a[28:31], a[16:19], v[72:87]\n"
    "s_add_u32 m0, s51, 48000\n"
    "s_nop 0\n"
    "buffer_load_dwordx4 v89, s[16:19], s57 offen lds\n"
    "s_add_u32 s12, s12, 1024\n"
    "ds_read_b64_tr_b16 a[12:13], v25 offset:27776\n"
    "ds_read_b64_tr_b16 a[14:15], v25 offset:28544\n"
    "s_waitcnt lgkmcnt(7)\n"
    "v_mfma_f32_32x32x16_bf16 v[40:55], a[36:39], a[32:35], v[40:55]\n"
    "s_add_u32 m0, s55, 113536\n"
    "s_mov_b64 exec, s[86:87]\n"
    "buffer_load_dwordx4 v93, s[20:23], s92 offen lds\n"
    "s_mov_b64 exec, -1\n"
    "ds_read_b128 a[16:19], v22 offset:32768\n"
    "ds_read_b64_tr_b16 a[20:21], v24 offset:30720\n"
    "ds_read_b64_tr_b16 a[22:23], v24 offset:31488\n"
    "v_mfma_f32_32x32x16_bf16 v[56:71], a[40:43], a[32:35], v[56:71]\n"
    "s_add_u32 m0, s52, 48000\n"
    "s_nop 0\n"
    "buffer_load_dwordx4 v90, s[16:19], s57 offen lds\n"
    "v_lshl_add_u32 v21, v2, 3, s12\n"
    "ds_read_b64_tr_b16 a[24:25], v24 offset:30784\n"
    "ds_read_b64_tr_b16 a[26:27], v24 offset:31552\n"
    "v_mfma_f32_32x32x16_bf16 v[72:87], a[44:47], a[32:35], v[72:87]\n"
    "ds_read_b64_tr_b16 a[28:29], v25 offset:30848\n"
    "ds_read_b64_tr_b16 a[30:31], v25 offset:31616\n"
    "s_waitcnt lgkmcnt(7)\n"
    "v_mfma_f32_32x32x16_bf16 v[40:55], a[4:7], a[0:3], v[40:55]\n"
    "s_add_u32 m0, s56, 113536\n"
    "s_mov_b64 exec, s[88:89]\n"
    "buffer_load_dwordx4 v94, s[20:23], s92 offen lds\n"
    "s_mov_b64 exec, -1\n"
    "v_add_u32 v30, s45, v6\n"
    "ds_read_b128 a[32:35], v23 offset:32768\n"
    "ds_read_b64_tr_b16 a[36:37], v24 offset:33792\n"
    "ds_read_b64_tr_b16 a[38:39], v24 offset:34560\n"
    "v_mfma_f32_32x32x16_bf16 v[56:71], a[8:11], a[0:3], v[56:71]\n"
    "s_add_u32 m0, s53, 48000\n"
    "s_nop 0\n"
    "buffer_load_dwordx4 v91, s[16:19], s57 offen lds\n"
    "ds_read_b64_tr_b16 a[40:41], v24 offset:33856\n"
    "ds_read_b64_tr_b16 a[42:43], v24 offset:34624\n"
    "v_mfma_f32_32x32x16_bf16 v[72:87], a[12:15], a[0:3], v[72:87]\n"
    "v_add_u32 v31, s46, v6\n"
    "ds_read_b64_tr_b16 a[44:45], v25 offset:33920\n"
    "ds_read_b64_tr_b16 a[46:47], v25 offset:34688\n"
    "s_waitcnt lgkmcnt(7)\n"
    "v_mfma_f32_32x32x16_bf16 v[40:55], a[20:23], a[16:19], v[40:55]\n"
    "s_waitcnt lgkmcnt(0)\n"
    "s_waitcnt vmcnt(7)\n"
    "s_barrier\n"
    "v_mfma_f32_32x32x16_bf16 v[56:71], a[24:27], a[16:19], v[56:71]\n"
    "s_add_u32 s59, s58, 128\n"
    "s_and_b32 s59, s59, 1023\n"
    "s_mul_i32 s94, s59, 1376\n"
    "s_add_u32 m0, s50, 80768\n"
    "s_nop 0\n"
    "buffer_load_dwordx4 v88, s[16:19], s59 offen lds\n"
    "ds_read_b128 a[0:3], v19 offset:49152\n"
    "ds_read_b64_tr_b16 a[4:5], v24 offset:36864\n"
    "ds_read_b64_tr_b16 a[6:7], v24 offset:37632\n"
    "ds_read_b64_tr_b16 a[8:9], v24 offset:36928\n"
    "v_mfma_f32_32x32x16_bf16 v[72:87], a[28:31], a[16:19], v[72:87]\n"
    "s_add_u32 m0, s54, 138112\n"
    "s_mov_b64 exec, s[84:85]\n"
    "buffer_load_dwordx4 v92, s[20:23], s94 offen lds\n"
    "s_mov_b64 exec, -1\n"
    "ds_read_b64_tr_b16 a[10:11], v24 offset:37696\n"
    "ds_read_b64_tr_b16 a[12:13], v25 offset:36992\n"
    "ds_read_b64_tr_b16 a[14:15], v25 offset:37760\n"
    "v_mfma_f32_32x32x16_bf16 v[40:55], a[36:39], a[32:35], v[40:55]\n"
    "ds_read_b128 a[16:19], v20 offset:49152\n"
    "ds_read_b64_tr_b16 a[20:21], v24 offset:39936\n"
    "ds_read_b64_tr_b16 a[22:23], v24 offset:40704\n"
    "v_mfma_f32_32x32x16_bf16 v[56:71], a[40:43], a[32:35], v[56:71]\n"
    "s_add_u32 m0, s51, 64384\n"
    "s_nop 0\n"
    "buffer_load_dwordx4 v89, s[16:19], s58 offen lds\n"
    "v_add_u32 v27, s90, v6\n"
    "ds_read_b64_tr_b16 a[24:25], v24 offset:40000\n"
    "ds_read_b64_tr_b16 a[26:27], v24 offset:40768\n"
    "v_mfma_f32_32x32x16_bf16 v[72:87], a[44:47], a[32:35], v[72:87]\n"
    "s_add_u32 m0, s55, 125824\n"
    "s_mov_b64 exec, s[86:87]\n"
    "buffer_load_dwordx4 v93, s[20:23], s93 offen lds\n"
    "s_mov_b64 exec, -1\n"
    "ds_read_b64_tr_b16 a[28:29], v25 offset:40064\n"
    "ds_read_b64_tr_b16 a[30:31], v25 offset:40832\n"
    "s_waitcnt lgkmcnt(7)\n"
    "v_mfma_f32_32x32x16_bf16 v[40:55], a[4:7], a[0:3], v[40:55]\n"
    "ds_read_b128 a[32:35], v22 offset:49152\n"
    "ds_read_b64_tr_b16 a[36:37], v24 offset:43008\n"
    "ds_read_b64_tr_b16 a[38:39], v24 offset:43776\n"
    "v_mfma_f32_32x32x16_bf16 v[56:71], a[8:11], a[0:3], v[56:71]\n"
    "s_add_u32 m0, s52, 64384\n"
    "s_nop 0\n"
    "buffer_load_dwordx4 v90, s[16:19], s58 offen lds\n"
    "ds_read_b64_tr_b16 a[40:41], v24 offset:43072\n"
    "ds_read_b64_tr_b16 a[42:43], v24 offset:43840\n"
    "v_mfma_f32_32x32x16_bf16 v[72:87], a[12:15], a[0:3], v[72:87]\n"
    "s_add_u32 m0, s56, 125824\n"
    "s_mov_b64 exec, s[88:89]\n"
    "buffer_load_dwordx4 v94, s[20:23], s93 offen lds\n"
    "s_mov_b64 exec, -1\n"
    "v_lshl_add_u32 v28, v2, 2, s91\n"
    "ds_read_b64_tr_b16 a[44:45], v25 offset:43136\n"
    "ds_read_b64_tr_b16 a[46:47], v25 offset:43904\n"
    "s_waitcnt lgkmcnt(7)\n"
    "v_mfma_f32_32x32x16_bf16 v[40:55], a[20:23], a[16:19], v[40:55]\n"
    "ds_read_b128 a[0:3], v23 offset:49152\n"
    "ds_read_b64_tr_b16 a[4:5], v24 offset:46080\n"
    "ds_read_b64_tr_b16 a[6:7], v24 offset:46848\n"
    "v_mfma_f32_32x32x16_bf16 v[56:71], a[24:27], a[16:19], v[56:71]\n"
    "s_add_u32 m0, s53, 64384\n"
    "s_nop 0\n"
    "buffer_load_dwordx4 v91, s[16:19], s58 offen lds\n"
    "ds_read_b64_tr_b16 a[8:9], v24 offset:46144\n"
    "ds_read_b64_tr_b16 a[10:11], v24 offset:46912\n"
    "v_mfma_f32_32x32x16_bf16 v[72:87], a[28:31], a[16:19], v[72:87]\n"
    "ds_read_b64_tr_b16 a[12:13], v25 offset:46208\n"
    "ds_read_b64_tr_b16 a[14:15], v25 offset:46976\n"
    "s_waitcnt lgkmcnt(7)\n"
    "v_mfma_f32_32x32x16_bf16 v[40:55], a[36:39], a[32:35], v[40:55]\n"
    "s_waitcnt lgkmcnt(0)\n"
    "s_waitcnt vmcnt(7)\n"
    "s_barrier\n"
    "v_mfma_f32_32x32x16_bf16 v[56:71], a[40:43], a[32:35], v[56:71]\n"
    "s_add_u32 s60, s59, 128\n"
    "s_and_b32 s60, s60, 1023\n"
    "s_mul_i32 s95, s60, 1376\n"
    "s_add_u32 m0, s50, 97152\n"
    "s_nop 0\n"
    "buffer_load_dwordx4 v88, s[16:19], s60 offen lds\n"
    "ds_read_b128 a[16:19], v19 offset:0\n"
    "ds_read_b64_tr_b16 a[20:21], v24 offset:0\n"
    "ds_read_b64_tr_b16 a[22:23], v24 offset:768\n"
    "ds_read_b64_tr_b16 a[24:25], v24 offset:64\n"
    "v_mfma_f32_32x32x16_bf16 v[72:87], a[44:47], a[32:35], v[72:87]\n"
    "s_add_u32 m0, s54, 150400\n"
    "s_mov_b64 exec, s[84:85]\n"
    "buffer_load_dwordx4 v92, s[20:23], s95 offen lds\n"
    "s_mov_b64 exec, -1\n"
    "ds_read_b64_tr_b16 a[26:27], v24 offset:832\n"
    "ds_read_b64_tr_b16 a[28:29], v26 offset:128\n"
    "ds_read_b64_tr_b16 a[30:31], v26 offset:896\n"
    "v_mfma_f32_32x32x16_bf16 v[40:55], a[4:7], a[0:3], v[40:55]\n"
    "s_add_u32 m0, s51, 80768\n"
    "s_nop 0\n"
    "buffer_load_dwordx4 v89, s[16:19], s59 offen lds\n"
    "ds_read_b128 a[32:35], v20 offset:0\n"
    "ds_read_b64_tr_b16 a[36:37], v24 offset:3072\n"
    "ds_read_b64_tr_b16 a[38:39], v24 offset:3840\n"
    "v_mfma_f32_32x32x16_bf16 v[56:71], a[8:11], a[0:3], v[56:71]\n"
    "ds_read_b64_tr_b16 a[40:41], v24 offset:3136\n"
    "ds_read_b64_tr_b16 a[42:43], v24 offset:3904\n"
    "v_mfma_f32_32x32x16_bf16 v[72:87], a[12:15], a[0:3], v[72:87]\n"
    "s_add_u32 m0, s55, 138112\n"
    "s_mov_b64 exec, s[86:87]\n"
    "buffer_load_dwordx4 v93, s[20:23], s94 offen lds\n"
    "s_mov_b64 exec, -1\n"
    "ds_read_b64_tr_b16 a[44:45], v26 offset:3200\n"
    "ds_read_b64_tr_b16 a[46:47], v26 offset:3968\n"
    "s_waitcnt lgkmcnt(7)\n"
    "v_mfma_f32_32x32x16_bf16 v[40:55], a[20:23], a[16:19], v[40:55]\n"
    "s_add_u32 m0, s52, 80768\n"
    "s_nop 0\n"
    "buffer_load_dwordx4 v90, s[16:19], s59 offen lds\n"
    "ds_read_b128 a[0:3], v22 offset:0\n"
    "ds_read_b64_tr_b16 a[4:5], v24 offset:6144\n"
    "ds_read_b64_tr_b16 a[6:7], v24 offset:6912\n"
    "v_mfma_f32_32x32x16_bf16 v[56:71], a[24:27], a[16:19], v[56:71]\n"
    "s_add_u32 m0, s56, 138112\n"
    "s_mov_b64 exec, s[88:89]\n"
    "buffer_load_dwordx4 v94, s[20:23], s94 offen lds\n"
    "s_mov_b64 exec, -1\n"
    "ds_read_b64_tr_b16 a[8:9], v24 offset:6208\n"
    "ds_read_b64_tr_b16 a[10:11], v24 offset:6976\n"
    "v_mfma_f32_32x32x16_bf16 v[72:87], a[28:31], a[16:19], v[72:87]\n"
    "ds_read_b64_tr_b16 a[12:13], v26 offset:6272\n"
    "ds_read_b64_tr_b16 a[14:15], v26 offset:7040\n"
    "s_waitcnt lgkmcnt(7)\n"
    "v_mfma_f32_32x32x16_bf16 v[40:55], a[36:39], a[32:35], v[40:55]\n"
    "s_add_u32 m0, s53, 80768\n"
    "s_nop 0\n"
    "buffer_load_dwordx4 v91, s[16:19], s59 offen lds\n"
    "ds_read_b128 a[16:19], v23 offset:0\n"
    "ds_read_b64_tr_b16 a[20:21], v24 offset:9216\n"
    "ds_read_b64_tr_b16 a[22:23], v24 offset:9984\n"
    "v_mfma_f32_32x32x16_bf16 v[56:71], a[40:43], a[32:35], v[56:71]\n"
    "buffer_load_dword v14, v21, s[24:27], 0 offen sc1\n"
    "ds_read_b64_tr_b16 a[24:25], v24 offset:9280\n"
    "ds_read_b64_tr_b16 a[26:27], v24 offset:10048\n"
    "v_mfma_f32_32x32x16_bf16 v[72:87], a[44:47], a[32:35], v[72:87]\n"
    "ds_read_b64_tr_b16 a[28:29], v26 offset:9344\n"
    "ds_read_b64_tr_b16 a[30:31], v26 offset:10112\n"
    "s_waitcnt lgkmcnt(7)\n"
    "v_mfma_f32_32x32x16_bf16 v[40:55], a[4:7], a[0:3], v[40:55]\n"
    "s_waitcnt lgkmcnt(0)\n"
    "s_waitcnt vmcnt(8)\n"
    "s_barrier\n"
    "v_mfma_f32_32x32x16_bf16 v[56:71], a[8:11], a[0:3], v[56:71]\n"
    "s_add_u32 m0, s51, 97152\n"
    "s_nop 0\n"
    "buffer_load_dwordx4 v89, s[16:19], s60 offen lds\n"
    "ds_read_b128 a[32:35], v19 offset:16384\n"
    "ds_read_b64_tr_b16 a[36:37], v24 offset:12288\n"
    "ds_read_b64_tr_b16 a[38:39], v24 offset:13056\n"
    "ds_read_b64_tr_b16 a[40:41], v24 offset:12352\n"
    "v_mfma_f32_32x32x16_bf16 v[72:87], a[12:15], a[0:3], v[72:87]\n"
    "ds_read_b64_tr_b16 a[42:43], v24 offset:13120\n"
    "ds_read_b64_tr_b16 a[44:45], v26 offset:12416\n"
    "ds_read_b64_tr_b16 a[46:47], v26 offset:13184\n"
    "v_mfma_f32_32x32x16_bf16 v[40:55], a[20:23], a[16:19], v[40:55]\n"
    "s_add_u32 m0, s55, 150400\n"
    "s_mov_b64 exec, s[86:87]\n"
    "buffer_load_dwordx4 v93, s[20:23], s95 offen lds\n"
    "s_mov_b64 exec, -1\n"
    "ds_read_b128 a[0:3], v20 offset:16384\n"
    "ds_read_b64_tr_b16 a[4:5], v24 offset:15360\n"
    "ds_read_b64_tr_b16 a[6:7], v24 offset:16128\n"
    "v_mfma_f32_32x32x16_bf16 v[56:71], a[24:27], a[16:19], v[56:71]\n"
    "ds_read_b64_tr_b16 a[8:9], v24 offset:15424\n"
    "ds_read_b64_tr_b16 a[10:11], v24 offset:16192\n"
    "v_mfma_f32_32x32x16_bf16 v[72:87], a[28:31], a[16:19], v[72:87]\n"
    "s_add_u32 m0, s52, 97152\n"
    "s_nop 0\n"
    "buffer_load_dwordx4 v90, s[16:19], s60 offen lds\n"
    "ds_read_b64_tr_b16 a[12:13], v26 offset:15488\n"
    "ds_read_b64_tr_b16 a[14:15], v26 offset:16256\n"
    "s_waitcnt lgkmcnt(7)\n"
    "v_mfma_f32_32x32x16_bf16 v[40:55], a[36:39], a[32:35], v[40:55]\n"
    "ds_read_b128 a[16:19], v22 offset:16384\n"
    "ds_read_b64_tr_b16 a[20:21], v24 offset:18432\n"
    "ds_read_b64_tr_b16 a[22:23], v24 offset:19200\n"
    "v_mfma_f32_32x32x16_bf16 v[56:71], a[40:43], a[32:35], v[56:71]\n"
    "s_add_u32 m0, s56, 150400\n"
    "s_mov_b64 exec, s[88:89]\n"
    "buffer_load_dwordx4 v94, s[20:23], s95 offen lds\n"
    "s_mov_b64 exec, -1\n"
    "ds_read_b64_tr_b16 a[24:25], v24 offset:18496\n"
    "ds_read_b64_tr_b16 a[26:27], v24 offset:19264\n"
    "v_mfma_f32_32x32x16_bf16 v[72:87], a[44:47], a[32:35], v[72:87]\n"
    "ds_read_b64_tr_b16 a[28:29], v26 offset:18560\n"
    "ds_read_b64_tr_b16 a[30:31], v26 offset:19328\n"
    "s_waitcnt lgkmcnt(7)\n"
    "v_mfma_f32_32x32x16_bf16 v[40:55], a[4:7], a[0:3], v[40:55]\n"
    "s_add_u32 m0, s53, 97152\n"
    "s_nop 0\n"
    "buffer_load_dwordx4 v91, s[16:19], s60 offen lds\n"
    "ds_read_b128 a[32:35], v23 offset:16384\n"
    "ds_read_b64_tr_b16 a[36:37], v24 offset:21504\n"
    "ds_read_b64_tr_b16 a[38:39], v24 offset:22272\n"
    "v_mfma_f32_32x32x16_bf16 v[56:71], a[8:11], a[0:3], v[56:71]\n"
    "ds_read_b64_tr_b16 a[40:41], v24 offset:21568\n"
    "ds_read_b64_tr_b16 a[42:43], v24 offset:22336\n"
    "v_mfma_f32_32x32x16_bf16 v[72:87], a[12:15], a[0:3], v[72:87]\n"
    "ds_read_b64_tr_b16 a[44:45], v26 offset:21632\n"
    "ds_read_b64_tr_b16 a[46:47], v26 offset:22400\n"
    "s_waitcnt lgkmcnt(7)\n"
    "v_mfma_f32_32x32x16_bf16 v[40:55], a[20:23], a[16:19], v[40:55]\n"
    "s_waitcnt lgkmcnt(0)\n"
    "s_waitcnt vmcnt(6)\n"
    "s_barrier\n"
    "s_waitcnt vmcnt(5)\n"
    "v_cmp_ne_u32 vcc, s80, v14\n"
    "s_nop 1\n"
    "s_cbranch_vccnz .Lfug_slow_%=\n"
    ".Lfug_ok_%=:\n"
    "v_mfma_f32_32x32x16_bf16 v[56:71], a[24:27], a[16:19], v[56:71]\n"
    "buffer_load_dwordx4 a[64:67], v30, s[24:27], 0 offen offset:0 sc1\n"
    "ds_read_b128 a[0:3], v19 offset:32768\n"
    "ds_read_b64_tr_b16 a[4:5], v24 offset:24576\n"
    "ds_read_b64_tr_b16 a[6:7], v24 offset:25344\n"
    "ds_read_b64_tr_b16 a[8:9], v24 offset:24640\n"
    "v_mfma_f32_32x32x16_bf16 v[72:87], a[28:31], a[16:19], v[72:87]\n"
    "buffer_load_dwordx4 a[68:71], v31, s[24:27], 0 offen offset:0 sc1\n"
    "ds_read_b64_tr_b16 a[10:11], v24 offset:25408\n"
    "ds_read_b64_tr_b16 a[12:13], v26 offset:24704\n"
    "ds_read_b64_tr_b16 a[14:15], v26 offset:25472\n"
    "v_mfma_f32_32x32x16_bf16 v[40:55], a[36:39], a[32:35], v[40:55]\n"
    "buffer_load_dwordx4 a[72:75], v30, s[24:27], 0 offen offset:1024 sc1\n"
    "ds_read_b128 a[16:19], v20 offset:32768\n"
    "ds_read_b64_tr_b16 a[20:21], v24 offset:27648\n"
    "ds_read_b64_tr_b16 a[22:23], v24 offset:28416\n"
    "v_mfma_f32_32x32x16_bf16 v[56:71], a[40:43], a[32:35], v[56:71]\n"
    "buffer_load_dwordx4 a[76:79], v31, s[24:27], 0 offen offset:1024 sc1\n"
    "ds_read_b64_tr_b16 a[24:25], v24 offset:27712\n"
    "ds_read_b64_tr_b16 a[26:27], v24 offset:28480\n"
    "v_mfma_f32_32x32x16_bf16 v[72:87], a[44:47], a[32:35], v[72:87]\n"
    "buffer_load_dwordx4 a[80:83], v30, s[24:27], 0 offen offset:2048 sc1\n"
    "ds_read_b64_tr_b16 a[28:29], v26 offset:27776\n"
    "ds_read_b64_tr_b16 a[30:31], v26 offset:28544\n"
    "s_waitcnt lgkmcnt(7)\n"
    "v_mfma_f32_32x32x16_bf16 v[40:55], a[4:7], a[0:3], v[40:55]\n"
    "buffer_load_dwordx4 a[84:87], v31, s[24:27], 0 offen offset:2048 sc1\n"
    "ds_read_b128 a[32:35], v22 offset:32768\n"
    "ds_read_b64_tr_b16 a[36:37], v24 offset:30720\n"
    "ds_read_b64_tr_b16 a[38:39], v24 offset:31488\n"
    "v_mfma_f32_32x32x16_bf16 v[56:71], a[8:11], a[0:3], v[56:71]\n"
    "buffer_load_dwordx4 a[88:91], v30, s[24:27], 0 offen offset:3072 sc1\n"
    "ds_read_b64_tr_b16 a[40:41], v24 offset:30784\n"
    "ds_read_b64_tr_b16 a[42:43], v24 offset:31552\n"
    "v_mfma_f32_32x32x16_bf16 v[72:87], a[12:15], a[0:3], v[72:87]\n"
    "buffer_load_dwordx4 a[92:95], v31, s[24:27], 0 offen offset:3072 sc1\n"
    "ds_read_b64_tr_b16 a[44:45], v26 offset:30848\n"
    "ds_read_b64_tr_b16 a[46:47], v26 offset:31616\n"
    "s_waitcnt lgkmcnt(7)\n"
    "v_mfma_f32_32x32x16_bf16 v[40:55], a[20:23], a[16:19], v[40:55]\n"
    "buffer_load_dwordx4 a[96:99], v30, s[24:27], s33 offen offset:0 sc1\n"
    "ds_read_b128 a[0:3], v23 offset:32768\n"
    "ds_read_b64_tr_b16 a[4:5], v24 offset:33792\n"
    "ds_read_b64_tr_b16 a[6:7], v24 offset:34560\n"
    "v_mfma_f32_32x32x16_bf16 v[56:71], a[24:27], a[16:19], v[56:71]\n"
    "buffer_load_dwordx4 a[100:103], v31, s[24:27], s33 offen offset:0 sc1\n"
    "ds_read_b64_tr_b16 a[8:9], v24 offset:33856\n"
    "ds_read_b64_tr_b16 a[10:11], v24 offset:34624\n"
    "v_mfma_f32_32x32x16_bf16 v[72:87], a[28:31], a[16:19], v[72:87]\n"
    "ds_read_b64_tr_b16 a[12:13], v26 offset:33920\n"
    "ds_read_b64_tr_b16 a[14:15], v26 offset:34688\n"
    "s_waitcnt lgkmcnt(7)\n"
    "v_mfma_f32_32x32x16_bf16 v[40:55], a[36:39], a[32:35], v[40:55]\n"
    "s_waitcnt lgkmcnt(0)\n"
    "s_waitcnt vmcnt(10)\n"
    "s_barrier\n"
    "v_mfma_f32_32x32x16_bf16 v[56:71], a[40:43], a[32:35], v[56:71]\n"
    "buffer_load_dwordx4 a[104:107], v30, s[24:27], s33 offen offset:1024 sc1\n"
    "ds_read_b128 a[16:19], v19 offset:49152\n"
    "ds_read_b64_tr_b16 a[20:21], v24 offset:36864\n"
    "ds_read_b64_tr_b16 a[22:23], v24 offset:37632\n"
    "ds_read_b64_tr_b16 a[24:25], v24 offset:36928\n"
    "v_mfma_f32_32x32x16_bf16 v[72:87], a[44:47], a[32:35], v[72:87]\n"
    "buffer_load_dwordx4 a[108:111], v31, s[24:27], s33 offen offset:1024 sc1\n"
    "ds_read_b64_tr_b16 a[26:27], v24 offset:37696\n"
    "ds_read_b64_tr_b16 a[28:29], v26 offset:36992\n"
    "ds_read_b64_tr_b16 a[30:31], v26 offset:37760\n"
    "v_mfma_f32_32x32x16_bf16 v[40:55], a[4:7], a[0:3], v[40:55]\n"
    "buffer_load_dwordx4 a[112:115], v30, s[24:27], s33 offen offset:2048 sc1\n"
    "ds_read_b128 a[32:35], v20 offset:49152\n"
    "ds_read_b64_tr_b16 a[36:37], v24 offset:39936\n"
    "ds_read_b64_tr_b16 a[38:39], v24 offset:40704\n"
    "v_mfma_f32_32x32x16_bf16 v[56:71], a[8:11], a[0:3], v[56:71]\n"
    "ds_read_b64_tr_b16 a[40:41], v24 offset:40000\n"
    "ds_read_b64_tr_b16 a[42:43], v24 offset:40768\n"
    "v_mfma_f32_32x32x16_bf16 v[72:87], a[12:15], a[0:3], v[72:87]\n"
    "buffer_load_dwordx4 a[116:119], v31, s[24:27], s33 offen offset:2048 sc1\n"
    "ds_read_b64_tr_b16 a[44:45], v26 offset:40064\n"
    "ds_read_b64_tr_b16 a[46:47], v26 offset:40832\n"
    "s_waitcnt lgkmcnt(7)\n"
    "v_mfma_f32_32x32x16_bf16 v[40:55], a[20:23], a[16:19], v[40:55]\n"
    "buffer_load_dwordx4 a[120:123], v30, s[24:27], s33 offen offset:3072 sc1\n"
    "ds_read_b128 a[0:3], v22 offset:49152\n"
    "ds_read_b64_tr_b16 a[4:5], v24 offset:43008\n"
    "ds_read_b64_tr_b16 a[6:7], v24 offset:43776\n"
    "v_mfma_f32_32x32x16_bf16 v[56:71], a[24:27], a[16:19], v[56:71]\n"
    "buffer_load_dwordx4 a[124:127], v31, s[24:27], s33 offen offset:3072 sc1\n"
    "ds_read_b64_tr_b16 a[8:9], v24 offset:43072\n"
    "ds_read_b64_tr_b16 a[10:11], v24 offset:43840\n"
    "v_mfma_f32_32x32x16_bf16 v[72:87], a[28:31], a[16:19], v[72:87]\n"
    "ds_read_b64_tr_b16 a[12:13], v26 offset:43136\n"
    "ds_read_b64_tr_b16 a[14:15], v26 offset:43904\n"
    "s_waitcnt lgkmcnt(7)\n"
    "v_mfma_f32_32x32x16_bf16 v[40:55], a[36:39], a[32:35], v[40:55]\n"
    "buffer_load_dwordx4 v[88:91], v30, s[24:27], s34 offen offset:0 sc1\n"
    "ds_read_b128 a[16:19], v23 offset:49152\n"
    "ds_read_b64_tr_b16 a[20:21], v24 offset:46080\n"
    "ds_read_b64_tr_b16 a[22:23], v24 offset:46848\n"
    "v_mfma_f32_32x32x16_bf16 v[56:71], a[40:43], a[32:35], v[56:71]\n"
    "buffer_load_dwordx4 v[92:95], v31, s[24:27], s34 offen offset:0 sc1\n"
    "ds_read_b64_tr_b16 a[24:25], v24 offset:46144\n"
    "ds_read_b64_tr_b16 a[26:27], v24 offset:46912\n"
    "v_mfma_f32_32x32x16_bf16 v[72:87], a[44:47], a[32:35], v[72:87]\n"
    "ds_read_b64_tr_b16 a[28:29], v26 offset:46208\n"
    "ds_read_b64_tr_b16 a[30:31], v26 offset:46976\n"
    "s_waitcnt lgkmcnt(7)\n"
    "v_mfma_f32_32x32x16_bf16 v[40:55], a[4:7], a[0:3], v[40:55]\n"
    "s_waitcnt lgkmcnt(0)\n"
    "s_barrier\n"
    "v_mfma_f32_32x32x16_bf16 v[56:71], a[8:11], a[0:3], v[56:71]\n"
    "v_mfma_f32_32x32x16_bf16 v[72:87], a[12:15], a[0:3], v[72:87]\n"
    "v_mfma_f32_32x32x16_bf16 v[40:55], a[20:23], a[16:19], v[40:55]\n"
    "v_mfma_f32_32x32x16_bf16 v[56:71], a[24:27], a[16:19], v[56:71]\n"
    "v_mfma_f32_32x32x16_bf16 v[72:87], a[28:31], a[16:19], v[72:87]\n"
    "s_add_u32 s12, s44, 13\n"
    "s_lshl_b32 s12, s12, 14\n"
    "s_lshl_b32 s13, s8, 7\n"
    "s_add_u32 s39, s12, s13\n"
    "s_mul_i32 s40, s3, 18432\n"
    "s_add_u32 s40, s40, 80768\n"
    "v_lshlrev_b32 v8, 6, v17\n"
    "v_xor_b32 v8, v8, v6\n"
    "v_and_b32 v8, 112, v8\n"
    "v_lshrrev_b32 v9, 3, v1\n"
    "v_lshl_or_b32 v19, v9, 10, v8\n"
    "v_max_f32 v40, 0, v40\n"
    "v_max_f32 v41, 0, v41\n"
    "v_max_f32 v42, 0, v42\n"
    "v_max_f32 v43, 0, v43\n"
    "v_max_f32 v44, 0, v44\n"
    "v_max_f32 v45, 0, v45\n"
    "v_max_f32 v46, 0, v46\n"
    "v_max_f32 v47, 0, v47\n"
    "v_mul_f32 v40, v40, v40\n"
    "v_mul_f32 v41, v41, v41\n"
    "v_mul_f32 v42, v42, v42\n"
    "v_mul_f32 v43, v43, v43\n"
    "v_mul_f32 v44, v44, v44\n"
    "v_mul_f32 v45, v45, v45\n"
    "v_mul_f32 v46, v46, v46\n"
    "v_mul_f32 v47, v47, v47\n"
    "v_cvt_pk_bf16_f32 v40, v40, v41\n"
    "v_cvt_pk_bf16_f32 v41, v42, v43\n"
    "v_cvt_pk_bf16_f32 v42, v44, v45\n"
    "v_cvt_pk_bf16_f32 v43, v46, v47\n"
    "buffer_store_dwordx4 v[40:43], v27, s[24:27], 0 offen offset:0 sc1\n"
    "buffer_load_dwordx4 v[96:99], v30, s[24:27], s34 offen offset:1024 sc1\n"
    "v_max_f32 v48, 0, v48\n"
    "v_max_f32 v49, 0, v49\n"
    "v_max_f32 v50, 0, v50\n"
    "v_max_f32 v51, 0, v51\n"
    "v_max_f32 v52, 0, v52\n"
    "v_max_f32 v53, 0, v53\n"
    "v_max_f32 v54, 0, v54\n"
    "v_max_f32 v55, 0, v55\n"
    "v_mul_f32 v48, v48, v48\n"
    "v_mul_f32 v49, v49, v49\n"
    "v_mul_f32 v50, v50, v50\n"
    "v_mul_f32 v51, v51, v51\n"
    "v_mul_f32 v52, v52, v52\n"
    "v_mul_f32 v53, v53, v53\n"
    "v_mul_f32 v54, v54, v54\n"
    "v_mul_f32 v55, v55, v55\n"
    "v_cvt_pk_bf16_f32 v48, v48, v49\n"
    "v_cvt_pk_bf16_f32 v49, v50, v51\n"
    "v_cvt_pk_bf16_f32 v50, v52, v53\n"
    "v_cvt_pk_bf16_f32 v51, v54, v55\n"
    "buffer_store_dwordx4 v[48:51], v27, s[24:27], 0 offen offset:1024 sc1\n"
    "buffer_load_dwordx4 v[100:103], v31, s[24:27], s34 offen offset:1024 sc1\n"
    "v_max_f32 v56, 0, v56\n"
    "v_max_f32 v57, 0, v57\n"
    "v_max_f32 v58, 0, v58\n"
    "v_max_f32 v59, 0, v59\n"
    "v_max_f32 v60, 0, v60\n"
    "v_max_f32 v61, 0, v61\n"
    "v_max_f32 v62, 0, v62\n"
    "v_max_f32 v63, 0, v63\n"
    "v_mul_f32 v56, v56, v56\n"
    "v_mul_f32 v57, v57, v57\n"
    "v_mul_f32 v58, v58, v58\n"
    "v_mul_f32 v59, v59, v59\n"
    "v_mul_f32 v60, v60, v60\n"
    "v_mul_f32 v61, v61, v61\n"
    "v_mul_f32 v62, v62, v62\n"
    "v_mul_f32 v63, v63, v63\n"
    "v_cvt_pk_bf16_f32 v56, v56, v57\n"
    "v_cvt_pk_bf16_f32 v57, v58, v59\n"
    "v_cvt_pk_bf16_f32 v58, v60, v61\n"
    "v_cvt_pk_bf16_f32 v59, v62, v63\n"
    "buffer_store_dwordx4 v[56:59], v27, s[24:27], 0 offen offset:2048 sc1\n"
    "buffer_load_dwordx4 v[104:107], v30, s[24:27], s34 offen offset:2048 sc1\n"
    "v_max_f32 v64, 0, v64\n"
    "v_max_f32 v65, 0, v65\n"
    "v_max_f32 v66, 0, v66\n"
    "v_max_f32 v67, 0, v67\n"
    "v_max_f32 v68, 0, v68\n"
    "v_max_f32 v69, 0, v69\n"
    "v_max_f32 v70, 0, v70\n"
    "v_max_f32 v71, 0, v71\n"
    "v_mul_f32 v64, v64, v64\n"
    "v_mul_f32 v65, v65, v65\n"
    "v_mul_f32 v66, v66, v66\n"
    "v_mul_f32 v67, v67, v67\n"
    "v_mul_f32 v68, v68, v68\n"
    "v_mul_f32 v69, v69, v69\n"
    "v_mul_f32 v70, v70, v70\n"
    "v_mul_f32 v71, v71, v71\n"
    "v_cvt_pk_bf16_f32 v64, v64, v65\n"
    "v_cvt_pk_bf16_f32 v65, v66, v67\n"
    "v_cvt_pk_bf16_f32 v66, v68, v69\n"
    "v_cvt_pk_bf16_f32 v67, v70, v71\n"
    "buffer_store_dwordx4 v[64:67], v27, s[24:27], 0 offen offset:3072 sc1\n"
    "buffer_load_dwordx4 v[108:111], v31, s[24:27], s34 offen offset:2048 sc1\n"
    "v_cvt_pk_bf16_f32 v10, v72, v72\n"
    "v_lshlrev_b32 v10, 16, v10\n"
    "v_mul_f32 v10, 0xbfb8aa3b, v10\n"
    "v_exp_f32 v10, v10\n"
    "s_nop 0\n"
    "v_add_f32 v10, 1.0, v10\n"
    "v_rcp_f32 v10, v10\n"
    "s_nop 0\n"
    "v_cvt_pk_bf16_f32 v10, v10, v10\n"
    "v_lshlrev_b32 v10, 16, v10\n"
    "buffer_store_dword v10, v28, s[24:27], 0 offen sc1\n"
    "v_cndmask_b32 v27, v27, v7, s[82:83]\n"
    "v_max_f32 v72, 0, v72\n"
    "v_max_f32 v73, 0, v73\n"
    "v_max_f32 v74, 0, v74\n"
    "v_max_f32 v75, 0, v75\n"
    "v_max_f32 v76, 0, v76\n"
    "v_max_f32 v77, 0, v77\n"
    "v_max_f32 v78, 0, v78\n"
    "v_max_f32 v79, 0, v79\n"
    "v_mul_f32 v72, v72, v72\n"
    "v_mul_f32 v73, v73, v73\n"
    "v_mul_f32 v74, v74, v74\n"
    "v_mul_f32 v75, v75, v75\n"
    "v_mul_f32 v76, v76, v76\n"
    "v_mul_f32 v77, v77, v77\n"
    "v_mul_f32 v78, v78, v78\n"
    "v_mul_f32 v79, v79, v79\n"
    "v_cvt_pk_bf16_f32 v72, v72, v73\n"
    "v_cvt_pk_bf16_f32 v73, v74, v75\n"
    "v_cvt_pk_bf16_f32 v74, v76, v77\n"
    "v_cvt_pk_bf16_f32 v75, v78, v79\n"
    "buffer_store_dwordx4 v[72:75], v27, s[24:27], s33 offen offset:0 sc1\n"
    "buffer_load_dwordx4 v[112:115], v30, s[24:27], s34 offen offset:3072 sc1\n"
    "v_max_f32 v80, 0, v80\n"
    "v_max_f32 v81, 0, v81\n"
    "v_max_f32 v82, 0, v82\n"
    "v_max_f32 v83, 0, v83\n"
    "v_max_f32 v84, 0, v84\n"
    "v_max_f32 v85, 0, v85\n"
    "v_max_f32 v86, 0, v86\n"
    "v_max_f32 v87, 0, v87\n"
    "v_mul_f32 v80, v80, v80\n"
    "v_mul_f32 v81, v81, v81\n"
    "v_mul_f32 v82, v82, v82\n"
    "v_mul_f32 v83, v83, v83\n"
    "v_mul_f32 v84, v84, v84\n"
    "v_mul_f32 v85, v85, v85\n"
    "v_mul_f32 v86, v86, v86\n"
    "v_mul_f32 v87, v87, v87\n"
    "v_cvt_pk_bf16_f32 v80, v80, v81\n"
    "v_cvt_pk_bf16_f32 v81, v82, v83\n"
    "v_cvt_pk_bf16_f32 v82, v84, v85\n"
    "v_cvt_pk_bf16_f32 v83, v86, v87\n"
    "buffer_store_dwordx4 v[80:83], v27, s[24:27], s33 offen offset:1024 sc1\n"
    "buffer_load_dwordx4 v[116:119], v31, s[24:27], s34 offen offset:3072 sc1\n"
    "buffer_load_dwordx4 v[120:123], v30, s[24:27], s35 offen offset:0 sc1\n"
    "s_waitcnt vmcnt(2)\n"
    "s_mov_b64 exec, 1\n"
    "buffer_store_dword v12, off, s[24:27], s43 sc1\n"
    "s_mov_b64 exec, -1\n"
    "buffer_load_dwordx4 v[124:127], v31, s[24:27], s35 offen offset:0 sc1\n"
    "s_lshr_b32 s13, s3, 1\n"
    "s_xor_b32 s13, s13, 0\n"
    "s_lshl_b32 s14, s7, 1\n"
    "s_add_u32 s13, s13, s14\n"
    "s_mul_i32 s13, s13, 86\n"
    "s_add_u32 s13, s13, s44\n"
    "s_lshl_b32 s13, s13, 10\n"
    "s_add_u32 s13, s13, 5767168\n"
    "v_add_u32 v27, s13, v6\n"
    "s_add_u32 m0, s40, 0\n"
    "s_nop 0\n"
    "buffer_load_dwordx4 v19, s[28:31], s39 offen lds\n"
    "s_add_u32 s39, s39, 8192\n"
    "s_lshr_b32 s13, s3, 1\n"
    "s_xor_b32 s13, s13, 1\n"
    "s_lshl_b32 s14, s7, 1\n"
    "s_add_u32 s13, s13, s14\n"
    "s_mul_i32 s13, s13, 86\n"
    "s_add_u32 s13, s13, s44\n"
    "s_lshl_b32 s13, s13, 10\n"
    "s_add_u32 s13, s13, 5767168\n"
    "v_add_u32 v28, s13, v6\n"
    "s_add_u32 m0, s40, 1024\n"
    "s_nop 0\n"
    "buffer_load_dwordx4 v19, s[28:31], s39 offen lds\n"
    "s_add_u32 s39, s39, 8192\n"
    "v_lshrrev_b32 v8, 1, v1\n"
    "s_add_u32 m0, s40, 2048\n"
    "s_nop 0\n"
    "buffer_load_dwordx4 v19, s[28:31], s39 offen lds\n"
    "s_add_u32 s39, s39, 8192\n"
    "v_lshl_add_u32 v8, v4, 2, v8\n"
    "v_min_u32 v8, 15, v8\n"
    "s_add_u32 m0, s40, 3072\n"
    "s_nop 0\n"
    "buffer_load_dwordx4 v19, s[28:31], s39 offen lds\n"
    "s_add_u32 s39, s39, 8192\n"
    "v_and_b32 v9, 1, v1\n"
    "s_add_u32 m0, s40, 4096\n"
    "s_nop 0\n"
    "buffer_load_dwordx4 v19, s[28:31], s39 offen lds\n"
    "s_add_u32 s39, s39, 8192\n"
    "v_lshl_add_u32 v8, v8, 2, v9\n"
    "s_add_u32 m0, s40, 5120\n"
    "s_nop 0\n"
    "buffer_load_dwordx4 v19, s[28:31], s39 offen lds\n"
    "s_add_u32 s39, s39, 8192\n"
    "s_lshr_b32 s12, s7, 1\n"
    "s_lshl_b32 s12, s12, 6\n"
    "s_and_b32 s13, s7, 1\n"
    "s_lshl_b32 s13, s13, 1\n"
    "s_add_u32 s12, s12, s13\n"
    "s_lshl_b32 s12, s12, 7\n"
    "s_add_u32 s12, s12, 32768\n"
    "v_lshl_add_u32 v29, v8, 7, s12\n"
    "v_cmp_lt_u32 vcc, 9, v1\n"
    "s_add_u32 m0, s40, 6144\n"
    "s_nop 0\n"
    "buffer_load_dwordx4 v19, s[28:31], s39 offen lds\n"
    "s_add_u32 s39, s39, 8192\n"
    "v_cndmask_b32 v29, v29, v7, vcc\n"
    "s_add_u32 m0, s40, 7168\n"
    "s_nop 0\n"
    "buffer_load_dwordx4 v19, s[28:31], s39 offen lds\n"
    "s_add_u32 s39, s39, 8192\n"
    "v_lshrrev_b32 v8, 1, v13\n"
    "s_and_b32 s12, s3, 1\n"
    "v_xor_b32 v8, s12, v8\n"
    "s_add_u32 m0, s40, 8192\n"
    "s_nop 0\n"
    "buffer_load_dwordx4 v19, s[28:31], s39 offen lds\n"
    "s_add_u32 s39, s39, 8192\n"
    "v_lshl_add_u32 v8, v8, 6, v18\n"
    "s_add_u32 m0, s40, 9216\n"
    "s_nop 0\n"
    "buffer_load_dwordx4 v19, s[28:31], s39 offen lds\n"
    "s_add_u32 s39, s39, 8192\n"
    "v_lshl_add_u32 v9, v3, 2, v13\n"
    "s_add_u32 m0, s40, 10240\n"
    "s_nop 0\n"
    "buffer_load_dwordx4 v19, s[28:31], s39 offen lds\n"
    "s_add_u32 s39, s39, 8192\n"
    "v_lshl_add_u32 v8, v9, 7, v8\n"
    "v_add_u32 v31, s40, v8\n"
    "s_add_u32 m0, s40, 11264\n"
    "s_nop 0\n"
    "buffer_load_dwordx4 v19, s[28:31], s39 offen lds\n"
    "s_add_u32 s39, s39, 8192\n"
    "v_xor_b32 v8, 64, v8\n"
    "s_add_u32 m0, s40, 12288\n"
    "s_nop 0\n"
    "buffer_load_dwordx4 v19, s[28:31], s39 offen lds\n"
    "s_add_u32 s39, s39, 8192\n"
    "v_add_u32 v20, s40, v8\n"
    "s_lshr_b32 s12, s3, 1\n"
    "s_and_b32 s13, s3, 1\n"
    "s_lshl_b32 s14, s7, 1\n"
    "s_add_u32 s14, s14, s12\n"
    "s_lshl_b32 s15, s8, 1\n"
    "s_add_u32 s15, s15, s13\n"
    "s_lshl_b32 s12, s15, 6\n"
    "s_lshl_b32 s13, s14, 15\n"
    "s_add_u32 s12, s12, s13\n"
    "v_lshl_add_u32 v21, v2, 10, s12\n"
    "s_add_u32 m0, s40, 13312\n"
    "s_nop 0\n"
    "buffer_load_dwordx4 v19, s[28:31], s39 offen lds\n"
    "s_add_u32 s39, s39, 8192\n"
    "v_lshl_add_u32 v21, v3, 4, v21\n"
    "s_add_u32 m0, s40, 14336\n"
    "s_nop 0\n"
    "buffer_load_dwordx4 v19, s[28:31], s39 offen lds\n"
    "s_add_u32 s39, s39, 8192\n"
    "s_xor_b32 s12, s3, 1\n"
    "s_mul_i32 s12, s12, 18432\n"
    "s_add_u32 s12, s12, 80768\n"
    "v_add_u32 v24, s12, v6\n"
    "s_add_u32 m0, s40, 15360\n"
    "s_nop 0\n"
    "buffer_load_dwordx4 v19, s[28:31], s39 offen lds\n"
    "s_add_u32 s39, s39, 8192\n"
    "s_xor_b32 s12, s3, 2\n"
    "s_mul_i32 s12, s12, 18432\n"
    "s_add_u32 s12, s12, 84864\n"
    "v_add_u32 v25, s12, v6\n"
    "s_xor_b32 s12, s3, 3\n"
    "s_mul_i32 s12, s12, 18432\n"
    "s_add_u32 s12, s12, 88960\n"
    "v_add_u32 v26, s12, v6\n"
    "v_cndmask_b32 v19, v19, v7, s[48:49]\n"
    "s_add_u32 m0, s40, 16384\n"
    "s_nop 0\n"
    "buffer_load_dwordx4 v19, s[28:31], s39 offen lds\n"
    "s_add_u32 s39, s39, 8192\n"
    "v_add_u32 v23, s40, v6\n"
    "s_add_u32 m0, s40, 17408\n"
    "s_nop 0\n"
    "buffer_load_dwordx4 v19, s[28:31], s39 offen lds\n"
    "s_add_u32 s39, s39, 8192\n"
    "s_mul_i32 s41, s3, 4352\n"
    "s_add_u32 s41, s41, 25984\n"
    "v_add_u32 v22, s41, v6\n"
    "v_mov_b32 v14, 0\n"
    "buffer_atomic_or v14, v29, s[24:27], 0 offen sc0 sc1\n"
    "s_waitcnt vmcnt(0)\n"
    "v_cmp_ne_u32 vcc, s80, v14\n"
    "s_nop 0\n"
    "s_and_b64 vcc, vcc, 0x3ff\n"
    "s_nop 1\n"
    "s_cbranch_vccnz .Lfh_slow_%=\n"
    ".Lfh_ok_%=:\n"
    "buffer_load_dwordx4 v[40:43], v27, s[24:27], 0 offen offset:0 sc1\n"
    "buffer_load_dwordx4 v[44:47], v28, s[24:27], 0 offen offset:0 sc1\n"
    "buffer_load_dwordx4 v[48:51], v27, s[24:27], 0 offen offset:1024 sc1\n"
    "buffer_load_dwordx4 v[52:55], v28, s[24:27], 0 offen offset:1024 sc1\n"
    "buffer_load_dwordx4 v[56:59], v27, s[24:27], 0 offen offset:2048 sc1\n"
    "buffer_load_dwordx4 v[60:63], v28, s[24:27], 0 offen offset:2048 sc1\n"
    "buffer_load_dwordx4 v[64:67], v27, s[24:27], 0 offen offset:3072 sc1\n"
    "buffer_load_dwordx4 v[68:71], v28, s[24:27], 0 offen offset:3072 sc1\n"
    "s_waitcnt vmcnt(7)\n"
    "v_mfma_f32_32x32x16_bf16 a[0:15], a[64:67], v[40:43], 0\n"
    "buffer_load_dwordx4 v[72:75], v27, s[24:27], s33 offen offset:0 sc1\n"
    "v_mfma_f32_32x32x16_bf16 a[16:31], a[68:71], v[40:43], 0\n"
    "s_waitcnt vmcnt(7)\n"
    "v_mfma_f32_32x32x16_bf16 a[32:47], a[64:67], v[44:47], 0\n"
    "buffer_load_dwordx4 v[76:79], v28, s[24:27], s33 offen offset:0 sc1\n"
    "v_mfma_f32_32x32x16_bf16 a[48:63], a[68:71], v[44:47], 0\n"
    "s_waitcnt vmcnt(7)\n"
    "v_mfma_f32_32x32x16_bf16 a[0:15], a[72:75], v[48:51], a[0:15]\n"
    "buffer_load_dwordx4 v[80:83], v27, s[24:27], s33 offen offset:1024 sc1\n"
    "v_mfma_f32_32x32x16_bf16 a[16:31], a[76:79], v[48:51], a[16:31]\n"
    "s_waitcnt vmcnt(7)\n"
    "v_mfma_f32_32x32x16_bf16 a[32:47], a[72:75], v[52:55], a[32:47]\n"
    "buffer_load_dwordx4 v[84:87], v28, s[24:27], s33 offen offset:1024 sc1\n"
    "v_mfma_f32_32x32x16_bf16 a[48:63], a[76:79], v[52:55], a[48:63]\n"
    "s_waitcnt vmcnt(7)\n"
    "v_mfma_f32_32x32x16_bf16 a[0:15], a[80:83], v[56:59], a[0:15]\n"
    "buffer_load_dwordx4 v[40:43], v27, s[24:27], s33 offen offset:2048 sc1\n"
    "v_mfma_f32_32x32x16_bf16 a[16:31], a[84:87], v[56:59], a[16:31]\n"
    "s_waitcnt vmcnt(7)\n"
    "v_mfma_f32_32x32x16_bf16 a[32:47], a[80:83], v[60:63], a[32:47]\n"
    "buffer_load_dwordx4 v[44:47], v28, s[24:27], s33 offen offset:2048 sc1\n"
    "v_mfma_f32_32x32x16_bf16 a[48:63], a[84:87], v[60:63], a[48:63]\n"
    "s_waitcnt vmcnt(7)\n"
    "v_mfma_f32_32x32x16_bf16 a[0:15], a[88:91], v[64:67], a[0:15]\n"
    "buffer_load_dwordx4 v[48:51], v27, s[24:27], s33 offen offset:3072 sc1\n"
    "v_mfma_f32_32x32x16_bf16 a[16:31], a[92:95], v[64:67], a[16:31]\n"
    "ds_read_b64_tr_b16 a[64:65], v31 offset:0\n"
    "ds_read_b64_tr_b16 a[66:67], v31 offset:1024\n"
    "s_waitcnt vmcnt(7)\n"
    "v_mfma_f32_32x32x16_bf16 a[32:47], a[88:91], v[68:71], a[32:47]\n"
    "buffer_load_dwordx4 v[52:55], v28, s[24:27], s33 offen offset:3072 sc1\n"
    "v_mfma_f32_32x32x16_bf16 a[48:63], a[92:95], v[68:71], a[48:63]\n"
    "ds_read_b64_tr_b16 a[68:69], v20 offset:0\n"
    "ds_read_b64_tr_b16 a[70:71], v20 offset:1024\n"
    "s_waitcnt vmcnt(7)\n"
    "v_mfma_f32_32x32x16_bf16 a[0:15], a[96:99], v[72:75], a[0:15]\n"
    "buffer_load_dwordx4 v[56:59], v27, s[24:27], s34 offen offset:0 sc1\n"
    "v_mfma_f32_32x32x16_bf16 a[16:31], a[100:103], v[72:75], a[16:31]\n"
    "ds_read_b64_tr_b16 a[72:73], v31 offset:2048\n"
    "ds_read_b64_tr_b16 a[74:75], v31 offset:3072\n"
    "s_waitcnt vmcnt(7)\n"
    "v_mfma_f32_32x32x16_bf16 a[32:47], a[96:99], v[76:79], a[32:47]\n"
    "buffer_load_dwordx4 v[60:63], v28, s[24:27], s34 offen offset:0 sc1\n"
    "v_mfma_f32_32x32x16_bf16 a[48:63], a[100:103], v[76:79], a[48:63]\n"
    "ds_read_b64_tr_b16 a[76:77], v20 offset:2048\n"
    "ds_read_b64_tr_b16 a[78:79], v20 offset:3072\n"
    "s_waitcnt vmcnt(7)\n"
    "v_mfma_f32_32x32x16_bf16 a[0:15], a[104:107], v[80:83], a[0:15]\n"
    "buffer_load_dwordx4 v[64:67], v27, s[24:27], s34 offen offset:1024 sc1\n"
    "v_mfma_f32_32x32x16_bf16 a[16:31], a[108:111], v[80:83], a[16:31]\n"
    "ds_read_b64_tr_b16 a[80:81], v31 offset:4096\n"
    "ds_read_b64_tr_b16 a[82:83], v31 offset:5120\n"
    "s_waitcnt vmcnt(7)\n"
    "v_mfma_f32_32x32x16_bf16 a[32:47], a[104:107], v[84:87], a[32:47]\n"
    "buffer_load_dwordx4 v[68:71], v28, s[24:27], s34 offen offset:1024 sc1\n"
    "v_mfma_f32_32x32x16_bf16 a[48:63], a[108:111], v[84:87], a[48:63]\n"
    "ds_read_b64_tr_b16 a[84:85], v20 offset:4096\n"
    "ds_read_b64_tr_b16 a[86:87], v20 offset:5120\n"
    "s_waitcnt vmcnt(7)\n"
    "v_mfma_f32_32x32x16_bf16 a[0:15], a[112:115], v[40:43], a[0:15]\n"
    "buffer_load_dwordx4 v[72:75], v27, s[24:27], s34 offen offset:2048 sc1\n"
    "v_mfma_f32_32x32x16_bf16 a[16:31], a[116:119], v[40:43], a[16:31]\n"
    "ds_read_b64_tr_b16 a[88:89], v31 offset:6144\n"
    "ds_read_b64_tr_b16 a[90:91], v31 offset:7168\n"
    "s_waitcnt vmcnt(7)\n"
    "v_mfma_f32_32x32x16_bf16 a[32:47], a[112:115], v[44:47], a[32:47]\n"
    "buffer_load_dwordx4 v[76:79], v28, s[24:27], s34 offen offset:2048 sc1\n"
    "v_mfma_f32_32x32x16_bf16 a[48:63], a[116:119], v[44:47], a[48:63]\n"
    "ds_read_b64_tr_b16 a[92:93], v20 offset:6144\n"
    "ds_read_b64_tr_b16 a[94:95], v20 offset:7168\n"
    "s_waitcnt vmcnt(7)\n"
    "v_mfma_f32_32x32x16_bf16 a[0:15], a[120:123], v[48:51], a[0:15]\n"
    "buffer_load_dwordx4 v[80:83], v27, s[24:27], s34 offen offset:3072 sc1\n"
    "v_mfma_f32_32x32x16_bf16 a[16:31], a[124:127], v[48:51], a[16:31]\n"
    "ds_read_b64_tr_b16 a[96:97], v31 offset:8192\n"
    "ds_read_b64_tr_b16 a[98:99], v31 offset:9216\n"
    "s_waitcnt vmcnt(7)\n"
    "v_mfma_f32_32x32x16_bf16 a[32:47], a[120:123], v[52:55], a[32:47]\n"
    "buffer_load_dwordx4 v[84:87], v28, s[24:27], s34 offen offset:3072 sc1\n"
    "v_mfma_f32_32x32x16_bf16 a[48:63], a[124:127], v[52:55], a[48:63]\n"
    "ds_read_b64_tr_b16 a[100:101], v20 offset:8192\n"
    "ds_read_b64_tr_b16 a[102:103], v20 offset:9216\n"
    "s_waitcnt vmcnt(7)\n"
    "v_mfma_f32_32x32x16_bf16 a[0:15], v[88:91], v[56:59], a[0:15]\n"
    "buffer_load_dwordx4 v[40:43], v27, s[24:27], s35 offen offset:0 sc1\n"
    "v_mfma_f32_32x32x16_bf16 a[16:31], v[92:95], v[56:59], a[16:31]\n"
    "ds_read_b64_tr_b16 a[104:105], v31 offset:10240\n"
    "ds_read_b64_tr_b16 a[106:107], v31 offset:11264\n"
    "s_waitcnt vmcnt(7)\n"
    "v_mfma_f32_32x32x16_bf16 a[32:47], v[88:91], v[60:63], a[32:47]\n"
    "buffer_load_dwordx4 v[44:47], v28, s[24:27], s35 offen offset:0 sc1\n"
    "v_mfma_f32_32x32x16_bf16 a[48:63], v[92:95], v[60:63], a[48:63]\n"
    "ds_read_b64_tr_b16 a[108:109], v20 offset:10240\n"
    "ds_read_b64_tr_b16 a[110:111], v20 offset:11264\n"
    "s_waitcnt vmcnt(7)\n"
    "v_mfma_f32_32x32x16_bf16 a[0:15], v[96:99], v[64:67], a[0:15]\n"
    "buffer_load_dwordx4 v[48:51], v27, s[24:27], s35 offen offset:1024 sc1\n"
    "v_mfma_f32_32x32x16_bf16 a[16:31], v[100:103], v[64:67], a[16:31]\n"
    "ds_read_b64_tr_b16 a[112:113], v31 offset:12288\n"
    "ds_read_b64_tr_b16 a[114:115], v31 offset:13312\n"
    "s_waitcnt vmcnt(7)\n"
    "v_mfma_f32_32x32x16_bf16 a[32:47], v[96:99], v[68:71], a[32:47]\n"
    "buffer_load_dwordx4 v[52:55], v28, s[24:27], s35 offen offset:1024 sc1\n"
    "v_mfma_f32_32x32x16_bf16 a[48:63], v[100:103], v[68:71], a[48:63]\n"
    "ds_read_b64_tr_b16 a[116:117], v20 offset:12288\n"
    "ds_read_b64_tr_b16 a[118:119], v20 offset:13312\n"
    "s_waitcnt vmcnt(7)\n"
    "v_mfma_f32_32x32x16_bf16 a[0:15], v[104:107], v[72:75], a[0:15]\n"
    "buffer_load_dwordx4 v[56:59], v27, s[24:27], s35 offen offset:2048 sc1\n"
    "v_mfma_f32_32x32x16_bf16 a[16:31], v[108:111], v[72:75], a[16:31]\n"
    "ds_read_b64_tr_b16 a[120:121], v31 offset:14336\n"
    "ds_read_b64_tr_b16 a[122:123], v31 offset:15360\n"
    "s_waitcnt vmcnt(7)\n"
    "v_mfma_f32_32x32x16_bf16 a[32:47], v[104:107], v[76:79], a[32:47]\n"
    "buffer_load_dwordx4 v[60:63], v28, s[24:27], s35 offen offset:2048 sc1\n"
    "v_mfma_f32_32x32x16_bf16 a[48:63], v[108:111], v[76:79], a[48:63]\n"
    "ds_read_b64_tr_b16 a[124:125], v20 offset:14336\n"
    "ds_read_b64_tr_b16 a[126:127], v20 offset:15360\n"
    "s_waitcnt vmcnt(7)\n"
    "v_mfma_f32_32x32x16_bf16 a[0:15], v[112:115], v[80:83], a[0:15]\n"
    "buffer_load_dwordx4 v[64:67], v27, s[24:27], s35 offen offset:3072 sc1\n"
    "v_mfma_f32_32x32x16_bf16 a[16:31], v[116:119], v[80:83], a[16:31]\n"
    "ds_read_b64_tr_b16 v[88:89], v31 offset:16384\n"
    "ds_read_b64_tr_b16 v[90:91], v31 offset:17408\n"
    "s_waitcnt vmcnt(7)\n"
    "v_mfma_f32_32x32x16_bf16 a[32:47], v[112:115], v[84:87], a[32:47]\n"
    "buffer_load_dwordx4 v[68:71], v28, s[24:27], s35 offen offset:3072 sc1\n"
    "v_mfma_f32_32x32x16_bf16 a[48:63], v[116:119], v[84:87], a[48:63]\n"
    "ds_read_b64_tr_b16 v[92:93], v20 offset:16384\n"
    "ds_read_b64_tr_b16 v[94:95], v20 offset:17408\n"
    "s_waitcnt vmcnt(7)\n"
    "v_mfma_f32_32x32x16_bf16 a[0:15], v[120:123], v[40:43], a[0:15]\n"
    "buffer_load_dwordx4 v[72:75], v28, s[24:27], s36 offen offset:0 sc1\n"
    "v_mfma_f32_32x32x16_bf16 a[16:31], v[124:127], v[40:43], a[16:31]\n"
    "s_waitcnt vmcnt(7)\n"
    "v_lshlrev_b32 v8, 2, v4\n"
    "v_add_u32 v8, 162688, v8\n"
    "v_mov_b32 v9, 1\n"
    "ds_write_b32 v8, v9\n"
    "v_mfma_f32_32x32x16_bf16 a[32:47], v[120:123], v[44:47], a[32:47]\n"
    "buffer_load_dwordx4 v[76:79], v28, s[24:27], s36 offen offset:1024 sc1\n"
    "v_mfma_f32_32x32x16_bf16 a[48:63], v[124:127], v[44:47], a[48:63]\n"
    "s_waitcnt vmcnt(7)\n"
    "s_waitcnt lgkmcnt(15)\n"
    "v_mfma_f32_32x32x16_bf16 a[0:15], a[64:67], v[48:51], a[0:15]\n"
    "buffer_load_dwordx4 v[80:83], v28, s[24:27], s36 offen offset:2048 sc1\n"
    "v_mfma_f32_32x32x16_bf16 a[16:31], a[68:71], v[48:51], a[16:31]\n"
    "s_waitcnt vmcnt(7)\n"
    "v_mfma_f32_32x32x16_bf16 a[32:47], a[64:67], v[52:55], a[32:47]\n"
    "buffer_load_dwordx4 v[84:87], v28, s[24:27], s36 offen offset:3072 sc1\n"
    "v_mfma_f32_32x32x16_bf16 a[48:63], a[68:71], v[52:55], a[48:63]\n"
    "s_waitcnt vmcnt(7)\n"
    "v_mfma_f32_32x32x16_bf16 a[0:15], a[72:75], v[56:59], a[0:15]\n"
    "buffer_load_dwordx4 v[40:43], v28, s[24:27], s37 offen offset:0 sc1\n"
    "v_mfma_f32_32x32x16_bf16 a[16:31], a[76:79], v[56:59], a[16:31]\n"
    "s_waitcnt vmcnt(7)\n"
    "v_mfma_f32_32x32x16_bf16 a[32:47], a[72:75], v[60:63], a[32:47]\n"
    "v_cndmask_b32 v28, v28, v7, s[48:49]\n"
    "buffer_load_dwordx4 v[44:47], v28, s[24:27], s37 offen offset:1024 sc1\n"
    "v_mfma_f32_32x32x16_bf16 a[48:63], a[76:79], v[60:63], a[48:63]\n"
    "s_waitcnt vmcnt(7)\n"
    "v_mfma_f32_32x32x16_bf16 a[0:15], a[80:83], v[64:67], a[0:15]\n"
    "buffer_load_dwordx4 v[48:51], v27, s[24:27], s36 offen offset:0 sc1\n"
    "v_mfma_f32_32x32x16_bf16 a[16:31], a[84:87], v[64:67], a[16:31]\n"
    "s_waitcnt vmcnt(7)\n"
    "v_mfma_f32_32x32x16_bf16 a[32:47], a[80:83], v[68:71], a[32:47]\n"
    "buffer_load_dwordx4 v[52:55], v27, s[24:27], s36 offen offset:1024 sc1\n"
    "v_mfma_f32_32x32x16_bf16 a[48:63], a[84:87], v[68:71], a[48:63]\n"
    "s_waitcnt vmcnt(7)\n"
    "v_mfma_f32_32x32x16_bf16 a[32:47], a[88:91], v[72:75], a[32:47]\n"
    "buffer_load_dwordx4 v[56:59], v27, s[24:27], s36 offen offset:2048 sc1\n"
    "v_mfma_f32_32x32x16_bf16 a[48:63], a[92:95], v[72:75], a[48:63]\n"
    "s_waitcnt vmcnt(7)\n"
    "v_mfma_f32_32x32x16_bf16 a[32:47], a[96:99], v[76:79], a[32:47]\n"
    "buffer_load_dwordx4 v[60:63], v27, s[24:27], s36 offen offset:3072 sc1\n"
    "v_mfma_f32_32x32x16_bf16 a[48:63], a[100:103], v[76:79], a[48:63]\n"
    "s_waitcnt vmcnt(7)\n"
    "s_waitcnt lgkmcnt(13)\n"
    "v_mfma_f32_32x32x16_bf16 a[32:47], a[104:107], v[80:83], a[32:47]\n"
    "buffer_load_dwordx4 v[64:67], v27, s[24:27], s37 offen offset:0 sc1\n"
    "v_mfma_f32_32x32x16_bf16 a[48:63], a[108:111], v[80:83], a[48:63]\n"
    "s_waitcnt vmcnt(7)\n"
    "s_waitcnt lgkmcnt(9)\n"
    "v_mfma_f32_32x32x16_bf16 a[32:47], a[112:115], v[84:87], a[32:47]\n"
    "v_cndmask_b32 v27, v27, v7, s[48:49]\n"
    "buffer_load_dwordx4 v[68:71], v27, s[24:27], s37 offen offset:1024 sc1\n"
    "v_mfma_f32_32x32x16_bf16 a[48:63], a[116:119], v[84:87], a[48:63]\n"
    "s_waitcnt vmcnt(7)\n"
    "s_waitcnt lgkmcnt(5)\n"
    "v_mfma_f32_32x32x16_bf16 a[32:47], a[120:123], v[40:43], a[32:47]\n"
    "v_mfma_f32_32x32x16_bf16 a[48:63], a[124:127], v[40:43], a[48:63]\n"
    "s_waitcnt vmcnt(6)\n"
    "s_waitcnt lgkmcnt(1)\n"
    "v_mfma_f32_32x32x16_bf16 a[32:47], v[88:91], v[44:47], a[32:47]\n"
    "v_mfma_f32_32x32x16_bf16 a[48:63], v[92:95], v[44:47], a[48:63]\n"
    "s_waitcnt vmcnt(5)\n"
    "v_mfma_f32_32x32x16_bf16 a[0:15], a[88:91], v[48:51], a[0:15]\n"
    "v_mfma_f32_32x32x16_bf16 a[16:31], a[92:95], v[48:51], a[16:31]\n"
    "s_waitcnt vmcnt(4)\n"
    "v_mfma_f32_32x32x16_bf16 a[0:15], a[96:99], v[52:55], a[0:15]\n"
    "s_nop 6\n"
    "ds_write_b128 v23, a[48:51] offset:8192\n"
    "v_mfma_f32_32x32x16_bf16 a[16:31], a[100:103], v[52:55], a[16:31]\n"
    "ds_write_b128 v23, a[52:55] offset:9216\n"
    "s_waitcnt vmcnt(3)\n"
    "v_mfma_f32_32x32x16_bf16 a[0:15], a[104:107], v[56:59], a[0:15]\n"
    "ds_write_b128 v23, a[56:59] offset:10240\n"
    "v_mfma_f32_32x32x16_bf16 a[16:31], a[108:111], v[56:59], a[16:31]\n"
    "ds_write_b128 v23, a[60:63] offset:11264\n"
    "s_waitcnt vmcnt(2)\n"
    "v_mfma_f32_32x32x16_bf16 a[0:15], a[112:115], v[60:63], a[0:15]\n"
    "ds_write_b128 v23, a[32:35] offset:4096\n"
    "v_mfma_f32_32x32x16_bf16 a[16:31], a[116:119], v[60:63], a[16:31]\n"
    "ds_write_b128 v23, a[36:39] offset:5120\n"
    "s_waitcnt vmcnt(1)\n"
    "v_mfma_f32_32x32x16_bf16 a[0:15], a[120:123], v[64:67], a[0:15]\n"
    "ds_write_b128 v23, a[40:43] offset:6144\n"
    "v_mfma_f32_32x32x16_bf16 a[16:31], a[124:127], v[64:67], a[16:31]\n"
    "ds_write_b128 v23, a[44:47] offset:7168\n"
    "s_waitcnt vmcnt(0)\n"
    "v_mfma_f32_32x32x16_bf16 a[16:31], v[92:95], v[68:71], a[16:31]\n"
    "v_mfma_f32_32x32x16_bf16 a[0:15], v[88:91], v[68:71], a[0:15]\n"
    "s_nop 10\n"
    "ds_write_b128 v23, a[16:19] offset:0\n"
    "ds_write_b128 v23, a[20:23] offset:1024\n"
    "ds_write_b128 v23, a[24:27] offset:2048\n"
    "ds_write_b128 v23, a[28:31] offset:3072\n"
    "v_accvgpr_read_b32 v40, a0\n"
    "v_accvgpr_read_b32 v41, a1\n"
    "v_accvgpr_read_b32 v42, a2\n"
    "v_accvgpr_read_b32 v43, a3\n"
    "v_accvgpr_read_b32 v44, a4\n"
    "v_accvgpr_read_b32 v45, a5\n"
    "v_accvgpr_read_b32 v46, a6\n"
    "v_accvgpr_read_b32 v47, a7\n"
    "v_accvgpr_read_b32 v48, a8\n"
    "v_accvgpr_read_b32 v49, a9\n"
    "v_accvgpr_read_b32 v50, a10\n"
    "v_accvgpr_read_b32 v51, a11\n"
    "v_accvgpr_read_b32 v52, a12\n"
    "v_accvgpr_read_b32 v53, a13\n"
    "v_accvgpr_read_b32 v54, a14\n"
    "v_accvgpr_read_b32 v55, a15\n"
    "v_lshl_add_u32 v8, v1, 2, s41\n"
    "s_waitcnt lgkmcnt(0)\n"
    "s_barrier\n"
    "ds_read_b32 v10, v8 offset:4096\n"
    "ds_read_b128 v[80:83], v24 offset:0\n"
    "ds_read_b128 v[96:99], v25 offset:0\n"
    "ds_read_b128 v[112:115], v26 offset:0\n"
    "ds_read_b128 v[56:59], v22 offset:0\n"
    "ds_read_b128 v[84:87], v24 offset:1024\n"
    "ds_read_b128 v[100:103], v25 offset:1024\n"
    "ds_read_b128 v[116:119], v26 offset:1024\n"
    "ds_read_b128 v[60:63], v22 offset:1024\n"
    "ds_read_b128 v[88:91], v24 offset:2048\n"
    "ds_read_b128 v[104:107], v25 offset:2048\n"
    "ds_read_b128 v[120:123], v26 offset:2048\n"
    "ds_read_b128 v[64:67], v22 offset:2048\n"
    "ds_read_b128 v[92:95], v24 offset:3072\n"
    "ds_read_b128 v[108:111], v25 offset:3072\n"
    "ds_read_b128 v[124:127], v26 offset:3072\n"
    "ds_read_b128 v[68:71], v22 offset:3072\n"
    "s_waitcnt lgkmcnt(12)\n"
    "v_mov_b32 v11, v10\n"
    "v_pk_add_f32 v[40:41], v[40:41], v[80:81]\n"
    "v_pk_add_f32 v[96:97], v[96:97], v[112:113]\n"
    "v_pk_add_f32 v[42:43], v[42:43], v[82:83]\n"
    "v_pk_add_f32 v[98:99], v[98:99], v[114:115]\n"
    "v_pk_add_f32 v[40:41], v[40:41], v[96:97]\n"
    "v_pk_add_f32 v[42:43], v[42:43], v[98:99]\n"
    "v_pk_fma_f32 v[40:41], v[40:41], v[10:11], v[56:57]\n"
    "v_pk_fma_f32 v[42:43], v[42:43], v[10:11], v[58:59]\n"
    "v_cvt_pk_bf16_f32 v72, v40, v41\n"
    "v_cvt_pk_bf16_f32 v73, v42, v43\n"
    "s_waitcnt lgkmcnt(8)\n"
    "v_pk_add_f32 v[44:45], v[44:45], v[84:85]\n"
    "v_pk_add_f32 v[100:101], v[100:101], v[116:117]\n"
    "v_pk_add_f32 v[46:47], v[46:47], v[86:87]\n"
    "v_pk_add_f32 v[102:103], v[102:103], v[118:119]\n"
    "v_pk_add_f32 v[44:45], v[44:45], v[100:101]\n"
    "v_pk_add_f32 v[46:47], v[46:47], v[102:103]\n"
    "v_pk_fma_f32 v[44:45], v[44:45], v[10:11], v[60:61]\n"
    "v_pk_fma_f32 v[46:47], v[46:47], v[10:11], v[62:63]\n"
    "v_cvt_pk_bf16_f32 v74, v44, v45\n"
    "v_cvt_pk_bf16_f32 v75, v46, v47\n"
    "s_nop 0\n"
    "v_permlane32_swap_b32 v72, v74\n"
    "v_permlane32_swap_b32 v73, v75\n"
    "buffer_store_dwordx4 v[72:75], v21, s[76:79], 0 offen offset:0\n"
    "s_waitcnt lgkmcnt(4)\n"
    "v_pk_add_f32 v[48:49], v[48:49], v[88:89]\n"
    "v_pk_add_f32 v[104:105], v[104:105], v[120:121]\n"
    "v_pk_add_f32 v[50:51], v[50:51], v[90:91]\n"
    "v_pk_add_f32 v[106:107], v[106:107], v[122:123]\n"
    "v_pk_add_f32 v[48:49], v[48:49], v[104:105]\n"
    "v_pk_add_f32 v[50:51], v[50:51], v[106:107]\n"
    "v_pk_fma_f32 v[48:49], v[48:49], v[10:11], v[64:65]\n"
    "v_pk_fma_f32 v[50:51], v[50:51], v[10:11], v[66:67]\n"
    "v_cvt_pk_bf16_f32 v76, v48, v49\n"
    "v_cvt_pk_bf16_f32 v77, v50, v51\n"
    "s_waitcnt lgkmcnt(0)\n"
    "v_pk_add_f32 v[52:53], v[52:53], v[92:93]\n"
    "v_pk_add_f32 v[108:109], v[108:109], v[124:125]\n"
    "v_pk_add_f32 v[54:55], v[54:55], v[94:95]\n"
    "v_pk_add_f32 v[110:111], v[110:111], v[126:127]\n"
    "v_pk_add_f32 v[52:53], v[52:53], v[108:109]\n"
    "v_pk_add_f32 v[54:55], v[54:55], v[110:111]\n"
    "v_pk_fma_f32 v[52:53], v[52:53], v[10:11], v[68:69]\n"
    "v_pk_fma_f32 v[54:55], v[54:55], v[10:11], v[70:71]\n"
    "v_cvt_pk_bf16_f32 v78, v52, v53\n"
    "v_cvt_pk_bf16_f32 v79, v54, v55\n"
    "s_nop 0\n"
    "v_permlane32_swap_b32 v76, v78\n"
    "v_permlane32_swap_b32 v77, v79\n"
    "buffer_store_dwordx4 v[76:79], v21, s[76:79], 0 offen offset:32\n"
    "s_branch .Lend_%=\n"
    ".Lfuc_slow_%=:\n"
    "s_mov_b32 s38, 32768\n"
    ".Lfuc_loop_%=:\n"
    "s_sleep 4\n"
    "buffer_load_dword v14, v25, s[24:27], 0 offen sc1\n"
    "s_waitcnt vmcnt(0)\n"
    "v_cmp_ne_u32 vcc, s80, v14\n"
    "s_nop 1\n"
    "s_cbranch_vccz .Lfuc_ok_%=\n"
    "s_sub_u32 s38, s38, 1\n"
    "s_cmp_lg_u32 s38, 0\n"
    "s_cbranch_scc1 .Lfuc_loop_%=\n"
    "s_branch .Lfuc_ok_%=\n"
    ".Lc2_slow_%=:\n"
    "s_mov_b32 s38, 1048576\n"
    ".Lc2_loop_%=:\n"
    "s_sleep 1\n"
    "ds_read_b32 v24, v22 offset:16\n"
    "s_waitcnt lgkmcnt(0)\n"
    "v_cmp_gt_u32 vcc, 1, v24\n"
    "s_nop 1\n"
    "s_cbranch_vccz .Lc2_ok_%=\n"
    "s_sub_u32 s38, s38, 1\n"
    "s_cmp_lg_u32 s38, 0\n"
    "s_cbranch_scc1 .Lc2_loop_%=\n"
    "s_branch .Lc2_ok_%=\n"
    ".Lep_%=:\n"
    "s_mov_b32 s38, 32768\n"
    "v_lshlrev_b32 v28, 5, v1\n"
    ".Lep_loop_%=:\n"
    "buffer_load_dwordx4 v[64:67], v28, s[24:27], 0 offen offset:1024 sc1\n"
    "buffer_load_dwordx4 v[68:71], v28, s[24:27], 0 offen offset:1040 sc1\n"
    "s_waitcnt vmcnt(0)\n"
    "v_cmp_ne_u32 vcc, s80, v64\n"
    "v_cmp_ne_u32 s[10:11], s80, v66\n"
    "s_nop 0\n"
    "s_or_b64 vcc, vcc, s[10:11]\n"
    "v_cmp_ne_u32 s[10:11], s80, v68\n"
    "s_nop 0\n"
    "s_or_b64 vcc, vcc, s[10:11]\n"
    "v_cmp_ne_u32 s[10:11], s80, v70\n"
    "s_nop 0\n"
    "s_or_b64 vcc, vcc, s[10:11]\n"
    "s_nop 1\n"
    "s_cbranch_vccz .Lep_go_%=\n"
    "s_sleep 2\n"
    "s_sub_u32 s38, s38, 1\n"
    "s_cmp_lg_u32 s38, 0\n"
    "s_cbranch_scc1 .Lep_loop_%=\n"
    ".Lep_go_%=:\n"
    "s_mov_b32 s12, 0\n"
    "s_mov_b64 exec, 1\n"
    "buffer_store_dword v12, off, s[24:27], s12 sc1\n"
    "s_mov_b64 exec, -1\n"
    "s_branch .Lep_done_%=\n"
    ".Lyg_slow_%=:\n"
    "s_mov_b32 s38, 1048576\n"
    ".Lyg_loop_%=:\n"
    "s_sleep 4\n"
    "ds_read_b32 v24, v22 offset:0\n"
    "s_waitcnt lgkmcnt(0)\n"
    "v_cmp_gt_u32 vcc, 1, v24\n"
    "s_nop 1\n"
    "s_cbranch_vccz .Lyg_ok_%=\n"
    "s_sub_u32 s38, s38, 1\n"
    "s_cmp_lg_u32 s38, 0\n"
    "s_cbranch_scc1 .Lyg_loop_%=\n"
    "s_branch .Lyg_ok_%=\n"
    ".Lfy_slow_%=:\n"
    "s_mov_b32 s38, 32768\n"
    ".Lfy_loop_%=:\n"
    "s_sleep 10\n"
    "buffer_load_dword v14, v13, s[24:27], 0 offen sc1\n"
    "s_waitcnt vmcnt(0)\n"
    "v_cmp_ne_u32 vcc, s80, v14\n"
    "s_nop 1\n"
    "s_cbranch_vccz .Lfy_ok_%=\n"
    "s_sub_u32 s38, s38, 1\n"
    "s_cmp_lg_u32 s38, 0\n"
    "s_cbranch_scc1 .Lfy_loop_%=\n"
    "s_branch .Lfy_ok_%=\n"
    ".Lfug_slow_%=:\n"
    "s_mov_b32 s38, 32768\n"
    ".Lfug_loop_%=:\n"
    "s_sleep 2\n"
    "buffer_load_dword v14, v21, s[24:27], 0 offen sc1\n"
    "s_waitcnt vmcnt(0)\n"
    "v_cmp_ne_u32 vcc, s80, v14\n"
    "s_nop 1\n"
    "s_cbranch_vccz .Lfug_ok_%=\n"
    "s_sub_u32 s38, s38, 1\n"
    "s_cmp_lg_u32 s38, 0\n"
    "s_cbranch_scc1 .Lfug_loop_%=\n"
    "s_branch .Lfug_ok_%=\n"
    ".Lfh_slow_%=:\n"
    "s_mov_b32 s38, 32768\n"
    ".Lfh_loop_%=:\n"
    "s_sleep 2\n"
    "v_mov_b32 v14, 0\n"
    "buffer_atomic_or v14, v29, s[24:27], 0 offen sc0 sc1\n"
    "s_waitcnt vmcnt(0)\n"
    "v_cmp_ne_u32 vcc, s80, v14\n"
    "s_nop 0\n"
    "s_and_b64 vcc, vcc, 0x3ff\n"
    "s_nop 1\n"
    "s_cbranch_vccz .Lfh_ok_%=\n"
    "s_sub_u32 s38, s38, 1\n"
    "s_cmp_lg_u32 s38, 0\n"
    "s_cbranch_scc1 .Lfh_loop_%=\n"
    "s_branch .Lfh_ok_%=\n"
    ".Lend_%=:\n"

    :
    : "{s[0:1]}"(__builtin_amdgcn_kernarg_segment_ptr()), "{s2}"(blockIdx.x), "{v0}"(threadIdx.x), "s"((unsigned)(size_t)lds)
    : "memory", "vcc", "scc", "v1", "v2", "v3", "v4", "v5", "v6", "v7", "v8", "v9", "v10", "v11", "v12", "v13", "v14", "v15", "v16", "v17", "v18", "v19", "v20", "v21", "v22", "v23", "v24", "v25", "v26", "v27", "v28", "v29", "v30", "v31", "v32", "v33", "v34", "v35", "v36", "v37", "v38", "v39", "v40", "v41", "v42", "v43", "v44", "v45", "v46", "v47", "v48", "v49", "v50", "v51", "v52", "v53", "v54", "v55", "v56", "v57", "v58", "v59", "v60", "v61", "v62", "v63", "v64", "v65", "v66", "v67", "v68", "v69", "v70", "v71", "v72", "v73", "v74", "v75", "v76", "v77", "v78", "v79", "v80", "v81", "v82", "v83", "v84", "v85", "v86", "v87", "v88", "v89", "v90", "v91", "v92", "v93", "v94", "v95", "v96", "v97", "v98", "v99", "v100", "v101", "v102", "v103", "v104", "v105", "v106", "v107", "v108", "v109", "v110", "v111", "v112", "v113", "v114", "v115", "v116", "v117", "v118", "v119", "v120", "v121", "v122", "v123", "v124", "v125", "v126", "v127", "a0", "a1", "a2", "a3", "a4", "a5", "a6", "a7", "a8", "a9", "a10", "a11", "a12", "a13", "a14", "a15", "a16", "a17", "a18", "a19", "a20", "a21", "a22", "a23", "a24", "a25", "a26", "a27", "a28", "a29", "a30", "a31", "a32", "a33", "a34", "a35", "a36", "a37", "a38", "a39", "a40", "a41", "a42", "a43", "a44", "a45", "a46", "a47", "a48", "a49", "a50", "a51", "a52", "a53", "a54", "a55", "a56", "a57", "a58", "a59", "a60", "a61", "a62", "a63", "a64", "a65", "a66", "a67", "a68", "a69", "a70", "a71", "a72", "a73", "a74", "a75", "a76", "a77", "a78", "a79", "a80", "a81", "a82", "a83", "a84", "a85", "a86", "a87", "a88", "a89", "a90", "a91", "a92", "a93", "a94", "a95", "a96", "a97", "a98", "a99", "a100", "a101", "a102", "a103", "a104", "a105", "a106", "a107", "a108", "a109", "a110", "a111", "a112", "a113", "a114", "a115", "a116", "a117", "a118", "a119", "a120", "a121", "a122", "a123", "a124", "a125", "a126", "a127", "s3", "s4", "s5", "s6", "s7", "s8", "s9", "s10", "s11", "s12", "s13", "s14", "s15", "s16", "s17", "s18", "s19", "s20", "s21", "s22", "s23", "s24", "s25", "s26", "s27", "s28", "s29", "s30", "s31", "s33", "s34", "s35", "s36", "s37", "s38", "s39", "s40", "s41", "s42", "s43", "s44", "s45", "s46", "s47", "s48", "s49", "s50", "s51", "s52", "s53", "s54", "s55", "s56", "s57", "s58", "s59", "s60", "s61", "s62", "s63", "s64", "s65", "s66", "s67", "s68", "s69", "s70", "s71", "s72", "s73", "s74", "s75", "s76", "s77", "s78", "s79", "s80", "s81", "s82", "s83", "s84", "s85", "s86", "s87", "s88", "s89", "s90", "s91", "s92", "s93", "s94", "s95", "s96", "s97");
}

extern "C" void kernel_launch(void* const* d_in, const int* in_sizes, int n_in, void* d_out, int out_size, void* d_ws,
                              size_t ws_size, hipStream_t stream)
{
  static const int which[16] = {0, 10, -1, 12, 1, 2, 3, 4, 5, 6, 7, 8, 11, 14, 13, -2};
  Args a;
  for (int i = 0; i < 16; ++i) {
    const int k = which[i];
    a.s[i].p = k >= 0 ? d_in[k] : k == -1 ? d_ws : d_out;
    a.s[i].n = k >= 0 ? 2u * (uint32_t)in_sizes[k] : k == -1 ? 11927552u : 2u * (uint32_t)out_size;
    a.s[i].f = 0x00020000u;
  }
  gated_mix_fused<<<256, 512, 0, stream>>>(a);
}
```
